```python
import torch

def _softmax(x, axis=-1):
    return torch.softmax(x, dim=axis)
def _gelu(x):
    return torch.nn.functional.gelu(x, approximate="tanh")
def _silu(x):
    return torch.nn.functional.silu(x)
def _sigmoid(x):
    return torch.sigmoid(x)
def _layernorm(x, axis=-1, eps=1e-5):
    m = x.mean(dim=axis, keepdim=True)
    v = x.var(dim=axis, keepdim=True, unbiased=False)
    return (x - m) / torch.sqrt(v + eps)
def _scatter_add(tgt, idx, src, axis=0):
    return tgt.clone().index_add_(axis, idx, src)
def _segment_sum(src, seg, n):
    out = torch.zeros((n,) + src.shape[1:], dtype=src.dtype)
    return out.index_add_(0, seg, src)
def _pad(x, pw):
    flat = tuple(v for pair in reversed(pw) for v in pair)
    return torch.nn.functional.pad(x, flat)

kernel_name = 'parallel_sum__spectral_fnet__dense_activation_relu__prompt'

def setup_inputs():

    _w = (lambda a: (lambda t: t.to(torch.bfloat16) if t.is_floating_point() else t)(torch.as_tensor(a)))
    def _A_setup():
        B, S, D = 1, 2048, 1024
        ns = torch.arange(S)[:, None] * torch.arange(S)[None, :]
        nd = torch.arange(D)[:, None] * torch.arange(D)[None, :]
        Ws_re = torch.cos(-2.0 * torch.pi * ns / S).to(float)
        Ws_im = torch.sin(-2.0 * torch.pi * ns / S).to(float)
        Wd_re = torch.cos(-2.0 * torch.pi * nd / D).to(float)
        Wd_im = torch.sin(-2.0 * torch.pi * nd / D).to(float)
        return {
            "x": _w((lambda *s: torch.randn(*s, dtype=torch.bfloat16) * 0.05)(B, S, D)),
            "dft_seq_re": _w(Ws_re),
            "dft_seq_im": _w(Ws_im),
            "dft_hid_re": _w(Wd_re),
            "dft_hid_im": _w(Wd_im),
        }
    _A_in = _A_setup()

    def _B_setup():
        B, S, D, Dff = 1, 2048, 1024, 3584
        return {
            "x": _w((lambda *s: torch.randn(*s, dtype=torch.bfloat16) * 0.05)(B, S, D)),
            "w1": _w((lambda *s: torch.randn(*s, dtype=torch.bfloat16) * 0.4)(D, Dff)),
            "w2": _w((lambda *s: torch.randn(*s, dtype=torch.bfloat16) * 0.4)(Dff, D)),
        }
    _B_in = _B_setup()

    return {
        "A_x": _A_in["x"],
        "A_dft_seq_re": _A_in["dft_seq_re"],
        "A_dft_seq_im": _A_in["dft_seq_im"],
        "A_dft_hid_re": _A_in["dft_hid_re"],
        "A_dft_hid_im": _A_in["dft_hid_im"],
        "B_x": _B_in["x"],
        "B_w1": _B_in["w1"],
        "B_w2": _B_in["w2"],
    }

def reference(A_x, A_dft_seq_re, A_dft_seq_im, A_dft_hid_re, A_dft_hid_im, B_x, B_w1, B_w2):
    def _A_ref(x, dft_seq_re, dft_seq_im, dft_hid_re, dft_hid_im):
        a = torch.einsum("bsd,ed->bse", x, dft_hid_re)
        b = torch.einsum("bsd,ed->bse", x, dft_hid_im)
        y = torch.einsum("ts,bse->bte", dft_seq_re, a) - torch.einsum("ts,bse->bte", dft_seq_im, b)
        return y

    def _B_ref(x, w1, w2):
        h = torch.einsum("bsd,df->bsf", x, w1)
        zero = torch.zeros_like(h)
        h = torch.maximum(h, zero)
        return torch.einsum("bsf,fd->bsd", h, w2)

    x = A_x
    xn = _layernorm(x, axis=-1)
    return x + _A_ref(xn, A_dft_seq_re, A_dft_seq_im, A_dft_hid_re, A_dft_hid_im) + _B_ref(xn, B_w1, B_w2)
```

```cpp
#include <hip/hip_runtime.h>
#include <hip/hip_bf16.h>
typedef unsigned short u16;
typedef short bf16x8 __attribute__((ext_vector_type(8)));
typedef float f32x4 __attribute__((ext_vector_type(4)));

constexpr int S = 2048, D = 1024, F = 3584, KT = 5632;

__device__ __forceinline__ float bf2f(u16 v) { return __uint_as_float(((unsigned)v) << 16); }
__device__ __forceinline__ u16 f2bf(float f) { __hip_bfloat16 b = __float2bfloat16(f); return *reinterpret_cast<u16*>(&b); }

__global__ __launch_bounds__(256) void ln_kernel(const u16* __restrict__ x, u16* __restrict__ xn) {
  int wid = threadIdx.x >> 6, lane = threadIdx.x & 63;
  int row = blockIdx.x * 4 + wid;
  const u16* xr = x + (size_t)row * D;
  float v[16];
  bf16x8 a = *(const bf16x8*)(xr + lane * 8);
  bf16x8 b = *(const bf16x8*)(xr + 512 + lane * 8);
  float s = 0;
  for (int i = 0; i < 8; ++i) { v[i] = bf2f((u16)a[i]); v[8 + i] = bf2f((u16)b[i]); s += v[i] + v[8 + i]; }
  for (int o = 32; o; o >>= 1) s += __shfl_xor(s, o);
  float m = s * (1.0f / D);
  float q = 0;
  for (int i = 0; i < 16; ++i) { float d = v[i] - m; q += d * d; }
  for (int o = 32; o; o >>= 1) q += __shfl_xor(q, o);
  float r = rsqrtf(q * (1.0f / D) + 1e-5f);
  bf16x8 oa, ob;
  for (int i = 0; i < 8; ++i) { oa[i] = (short)f2bf((v[i] - m) * r); ob[i] = (short)f2bf((v[8 + i] - m) * r); }
  *(bf16x8*)(xn + (size_t)row * D + lane * 8) = oa;
  *(bf16x8*)(xn + (size_t)row * D + 512 + lane * 8) = ob;
}

template <bool BKN>
__device__ void gemm_tile(const u16* A, int lda, const u16* B, int ldb, int K, int m0, int n0, f32x4 (&acc)[2][2], u16* sA, u16* sB) {
  int tid = threadIdx.x, wid = tid >> 6, lane = tid & 63;
  int wr = wid >> 1, wc = wid & 1;
  for (int k0 = 0; k0 < K; k0 += 32) {
    { int r = tid >> 2, c = (tid & 3) * 8; *(bf16x8*)(sA + r * 40 + c) = *(const bf16x8*)(A + (size_t)(m0 + r) * lda + k0 + c); }
    if (BKN) { int k = tid >> 3, c = (tid & 7) * 8; bf16x8 v = *(const bf16x8*)(B + (size_t)(k0 + k) * ldb + n0 + c);
      for (int i = 0; i < 8; ++i) sB[(c + i) * 40 + k] = (u16)v[i]; }
    else { int r = tid >> 2, c = (tid & 3) * 8; *(bf16x8*)(sB + r * 40 + c) = *(const bf16x8*)(B + (size_t)(n0 + r) * ldb + k0 + c); }
    __syncthreads();
    bf16x8 af[2], bfr[2];
    for (int i = 0; i < 2; ++i) af[i] = *(bf16x8*)(sA + (wr * 32 + i * 16 + (lane & 15)) * 40 + (lane >> 4) * 8);
    for (int i = 0; i < 2; ++i) bfr[i] = *(bf16x8*)(sB + (wc * 32 + i * 16 + (lane & 15)) * 40 + (lane >> 4) * 8);
    for (int i = 0; i < 2; ++i) for (int j = 0; j < 2; ++j)
      acc[i][j] = __builtin_amdgcn_mfma_f32_16x16x32_bf16(af[i], bfr[j], acc[i][j], 0, 0, 0);
    __syncthreads();
  }
}

__global__ __launch_bounds__(256) void stage1(const u16* xn, const u16* Cr, const u16* Ci, const u16* w1, u16* T) {
  __shared__ u16 sA[64 * 40], sB[64 * 40];
  int bm = blockIdx.y, bn = blockIdx.x;
  int m0 = bm * 64, nT = bn * 64;
  f32x4 acc[2][2] = {};
  int mode;
  if (nT < 1024) { gemm_tile<true>(Cr, S, xn, D, S, m0, nT, acc, sA, sB); mode = 0; }
  else if (nT < 2048) { gemm_tile<true>(Ci, S, xn, D, S, m0, nT - 1024, acc, sA, sB); mode = 1; }
  else { gemm_tile<true>(xn, D, w1, F, D, m0, nT - 2048, acc, sA, sB); mode = 2; }
  int tid = threadIdx.x, wid = tid >> 6, lane = tid & 63; int wr = wid >> 1, wc = wid & 1;
  for (int i = 0; i < 2; ++i) for (int j = 0; j < 2; ++j) for (int r = 0; r < 4; ++r) {
    float v = acc[i][j][r];
    if (mode == 1) v = -v;
    if (mode == 2) v = fmaxf(v, 0.f);
    T[(size_t)(m0 + wr * 32 + i * 16 + (lane >> 4) * 4 + r) * KT + nT + wc * 32 + j * 16 + (lane & 15)] = f2bf(v);
  }
}

__global__ __launch_bounds__(256) void stage2(const u16* T, const u16* Dr, const u16* Di, const u16* w2, const u16* x, u16* out) {
  __shared__ u16 sA[64 * 40], sB[64 * 40];
  int m0 = blockIdx.y * 64, n0 = blockIdx.x * 64;
  f32x4 acc[2][2] = {};
  gemm_tile<false>(T, KT, Dr, D, 1024, m0, n0, acc, sA, sB);
  gemm_tile<false>(T + 1024, KT, Di, D, 1024, m0, n0, acc, sA, sB);
  gemm_tile<true>(T + 2048, KT, w2, D, F, m0, n0, acc, sA, sB);
  int tid = threadIdx.x, wid = tid >> 6, lane = tid & 63; int wr = wid >> 1, wc = wid & 1;
  for (int i = 0; i < 2; ++i) for (int j = 0; j < 2; ++j) for (int r = 0; r < 4; ++r) {
    size_t idx = (size_t)(m0 + wr * 32 + i * 16 + (lane >> 4) * 4 + r) * D + n0 + wc * 32 + j * 16 + (lane & 15);
    out[idx] = f2bf(acc[i][j][r] + bf2f(x[idx]));
  }
}

extern "C" void kernel_launch(void* const* d_in, const int* in_sizes, int n_in, void* d_out, int out_size, void* d_ws, size_t ws_size, hipStream_t stream) {
  const u16* x = (const u16*)d_in[0];
  const u16* Cr = (const u16*)d_in[1];
  const u16* Ci = (const u16*)d_in[2];
  const u16* Dr = (const u16*)d_in[3];
  const u16* Di = (const u16*)d_in[4];
  const u16* w1 = (const u16*)d_in[6];
  const u16* w2 = (const u16*)d_in[7];
  u16* xn = (u16*)d_ws;
  u16* T = xn + (size_t)S * D;
  ln_kernel<<<S / 4, 256, 0, stream>>>(x, xn);
  stage1<<<dim3(KT / 64, S / 64), 256, 0, stream>>>(xn, Cr, Ci, w1, T);
  stage2<<<dim3(D / 64, S / 64), 256, 0, stream>>>(T, Dr, Di, w2, x, (u16*)d_out);
}
```

```cpp
#include <hip/hip_runtime.h>
typedef unsigned short u16;
typedef unsigned int u32;
typedef unsigned long long u64;

struct Orders { u64 w[64]; };

__global__ __launch_bounds__(256, 1) void fused_kernel(const u16* x, const u16* Cr, const u16* Ci, const u16* Dr, const u16* Di,
                                                       const u16* w1, const u16* w2, u16* out, char* ws, Orders ord) {
  const u32 bid = blockIdx.x, tid = threadIdx.x;
  const u32 xcd = bid & 7, idx = bid >> 3;
  u32 role, pt, cb, pm, pn;
  role = (idx & 1) * 2 + (xcd & 1);
  { const u32 t = idx >> 1, q = xcd >> 1;
    pt = (q >> 1) * 4 + (t >> 2); cb = (q & 1) * 4 + (t & 3);
    pm = (xcd >> 2) * 4 + (t >> 2); pn = (xcd & 3) * 4 + (t & 3); }
  const u32 word = role | (pt << 2) | (cb << 5) | (pm << 8) | (pn << 11);
  const void* ka = (const void*)__builtin_amdgcn_kernarg_segment_ptr();
  asm volatile(
      "s_load_dwordx16 s[36:51], %0, 0x0\n"
      "s_load_dwordx2 s[52:53], %0, 0x40\n"
      "s_and_b32 s56, %1, 3\n"
      "s_bfe_u32 s57, %1, 0x30002\n"
      "s_bfe_u32 s58, %1, 0x30005\n"
      "s_bfe_u32 s59, %1, 0x30008\n"
      "s_bfe_u32 s60, %1, 0x4000b\n"
      "s_mov_b32 s87, %2\n"
      "v_and_b32 v4, 63, %3\n"
      "v_lshrrev_b32 v5, 6, %3\n"
      "v_lshlrev_b32 v6, 7, v4\n"
      "v_readfirstlane_b32 s61, v5\n"
      "s_getpc_b64 s[24:25]\n"
      "Lk_pc:\n"
      "s_and_b32 s25, s25, 0xffff\n"
      "s_lshr_b32 s72, s87, 3\n"
      "s_lshl_b32 s72, s72, 2\n"
      "s_add_u32 s72, s72, s61\n"
      "s_mul_i32 s72, s72, 384\n"
      "s_add_u32 s26, s72, 384\n"
      "s_min_u32 s26, s26, Lk_fin-Lk_pc\n"
      "s_mov_b32 s27, 0x20000\n"
      "buffer_load_dword a254, v6, s[24:27], s72 offen\n"
      "s_lshr_b32 s62, s61, 1\n"
      "s_and_b32 s63, s61, 1\n"
      "s_lshl_b32 s92, s61, 12\n"
      "s_lshl_b32 s85, s61, 13\n"
      "v_mov_b32 v5, 0\n"
      "s_lshl_b32 s72, s87, 14\n"
      "s_add_u32 s72, s72, s92\n"
      "v_lshlrev_b32 v160, 4, v4\n"
      "v_add_u32 v160, s72, v160\n"
      "s_waitcnt lgkmcnt(0)\n"
      "s_mov_b32 s68, s36\n"
      "s_and_b32 s69, s37, 0xffff\n"
      "s_mov_b32 s70, 0x400000\n"
      "s_mov_b32 s71, 0x20000\n"
      "buffer_load_dwordx4 v[64:67], v160, s[68:71], 0 offen offset:0\n"
      "buffer_load_dwordx4 v[68:71], v160, s[68:71], 0 offen offset:1024\n"
      "buffer_load_dwordx4 v[72:75], v160, s[68:71], 0 offen offset:2048\n"
      "buffer_load_dwordx4 v[76:79], v160, s[68:71], 0 offen offset:3072\n"
      "s_mov_b32 s64, s52\n"
      "s_and_b32 s65, s53, 0xffff\n"
      "s_mov_b32 s66, 0x3000000\n"
      "s_mov_b32 s67, 0x20000\n"
      "buffer_load_dwordx2 v[162:163], v5, s[64:67], 0 offen sc1\n"
      "s_lshl_b32 s72, s87, 2\n"
      "s_add_u32 s72, s72, s61\n"
      "s_mul_i32 s72, s72, 384\n"
      "s_add_u32 s72, s72, 0x20000\n"
      "s_mov_b64 s[20:21], s[64:65]\n"
      "s_mov_b64 s[22:23], s[66:67]\n"
      "s_add_u32 s22, s72, 384\n"
      "buffer_load_dword a253, v6, s[20:23], s72 offen sc1\n"
      "s_mul_i32 s86, s61, 7168\n"
      "s_mov_b32 s77, s58\n"
      "s_lshl_b32 s5, s57, 1\n"
      "s_add_u32 s5, s5, s56\n"
      "s_lshl_b32 s77, s77, 3\n"
      "s_lshl_b32 s72, s5, 3\n"
      "s_add_u32 s72, s72, 72\n"
      "s_lshl_b32 s73, s59, 3\n"
      "s_add_u32 s73, s73, 200\n"
      "s_lshl_b32 s74, s58, 3\n"
      "s_lshl_b32 s75, s57, 3\n"
      "s_cmp_lt_u32 s56, 2\n"
      "s_cselect_b32 s72, s72, s73\n"
      "s_cselect_b32 s73, 0, 64\n"
      "s_load_dwordx2 s[12:13], %0, s72\n"
      "s_add_u32 s76, s77, 264\n"
      "s_load_dwordx2 s[14:15], %0, s76\n"
      "s_add_u32 s74, s74, s73\n"
      "s_add_u32 s74, s74, 328\n"
      "s_load_dwordx2 s[16:17], %0, s74\n"
      "s_add_u32 s75, s75, s73\n"
      "s_add_u32 s75, s75, 456\n"
      "s_load_dwordx2 s[18:19], %0, s75\n"
      "s_waitcnt vmcnt(4)\n"
      "v_lshlrev_b32 v96, 16, v64\n"
      "v_and_b32 v97, 0xffff0000, v64\n"
      "v_lshlrev_b32 v98, 16, v65\n"
      "v_and_b32 v99, 0xffff0000, v65\n"
      "v_lshlrev_b32 v100, 16, v66\n"
      "v_and_b32 v101, 0xffff0000, v66\n"
      "v_lshlrev_b32 v102, 16, v67\n"
      "v_and_b32 v103, 0xffff0000, v67\n"
      "v_lshlrev_b32 v104, 16, v68\n"
      "v_and_b32 v105, 0xffff0000, v68\n"
      "v_lshlrev_b32 v106, 16, v69\n"
      "v_and_b32 v107, 0xffff0000, v69\n"
      "v_lshlrev_b32 v108, 16, v70\n"
      "v_and_b32 v109, 0xffff0000, v70\n"
      "v_lshlrev_b32 v110, 16, v71\n"
      "v_and_b32 v111, 0xffff0000, v71\n"
      "v_pk_add_f32 v[164:165], v[96:97], v[98:99]\n"
      "v_pk_add_f32 v[164:165], v[164:165], v[100:101]\n"
      "v_pk_add_f32 v[164:165], v[164:165], v[102:103]\n"
      "v_pk_add_f32 v[164:165], v[164:165], v[104:105]\n"
      "v_pk_add_f32 v[164:165], v[164:165], v[106:107]\n"
      "v_pk_add_f32 v[164:165], v[164:165], v[108:109]\n"
      "v_pk_add_f32 v[164:165], v[164:165], v[110:111]\n"
      "s_waitcnt vmcnt(2)\n"
      "v_lshlrev_b32 v112, 16, v72\n"
      "v_and_b32 v113, 0xffff0000, v72\n"
      "v_lshlrev_b32 v114, 16, v73\n"
      "v_and_b32 v115, 0xffff0000, v73\n"
      "v_lshlrev_b32 v116, 16, v74\n"
      "v_and_b32 v117, 0xffff0000, v74\n"
      "v_lshlrev_b32 v118, 16, v75\n"
      "v_and_b32 v119, 0xffff0000, v75\n"
      "v_lshlrev_b32 v120, 16, v76\n"
      "v_and_b32 v121, 0xffff0000, v76\n"
      "v_lshlrev_b32 v122, 16, v77\n"
      "v_and_b32 v123, 0xffff0000, v77\n"
      "v_lshlrev_b32 v124, 16, v78\n"
      "v_and_b32 v125, 0xffff0000, v78\n"
      "v_lshlrev_b32 v126, 16, v79\n"
      "v_and_b32 v127, 0xffff0000, v79\n"
      "v_pk_add_f32 v[166:167], v[112:113], v[114:115]\n"
      "v_pk_add_f32 v[166:167], v[166:167], v[116:117]\n"
      "v_pk_add_f32 v[166:167], v[166:167], v[118:119]\n"
      "v_pk_add_f32 v[166:167], v[166:167], v[120:121]\n"
      "v_pk_add_f32 v[166:167], v[166:167], v[122:123]\n"
      "v_pk_add_f32 v[166:167], v[166:167], v[124:125]\n"
      "v_pk_add_f32 v[166:167], v[166:167], v[126:127]\n"
      "v_add_f32 v164, v164, v165\n"
      "v_add_f32 v166, v166, v167\n"
      "s_nop 0\n"
      "v_add_f32_dpp v164, v164, v164 row_shr:1 row_mask:0xf bank_mask:0xf bound_ctrl:0\n"
      "v_add_f32_dpp v166, v166, v166 row_shr:1 row_mask:0xf bank_mask:0xf bound_ctrl:0\n"
      "s_nop 0\n"
      "v_add_f32_dpp v164, v164, v164 row_shr:2 row_mask:0xf bank_mask:0xf bound_ctrl:0\n"
      "v_add_f32_dpp v166, v166, v166 row_shr:2 row_mask:0xf bank_mask:0xf bound_ctrl:0\n"
      "s_nop 0\n"
      "v_add_f32_dpp v164, v164, v164 row_shr:4 row_mask:0xf bank_mask:0xf bound_ctrl:0\n"
      "v_add_f32_dpp v166, v166, v166 row_shr:4 row_mask:0xf bank_mask:0xf bound_ctrl:0\n"
      "s_nop 0\n"
      "v_add_f32_dpp v164, v164, v164 row_shr:8 row_mask:0xf bank_mask:0xf bound_ctrl:0\n"
      "v_add_f32_dpp v166, v166, v166 row_shr:8 row_mask:0xf bank_mask:0xf bound_ctrl:0\n"
      "s_nop 0\n"
      "v_add_f32_dpp v164, v164, v164 row_bcast:15 row_mask:0xa bank_mask:0xf\n"
      "v_add_f32_dpp v166, v166, v166 row_bcast:15 row_mask:0xa bank_mask:0xf\n"
      "s_nop 0\n"
      "v_add_f32_dpp v164, v164, v164 row_bcast:31 row_mask:0xc bank_mask:0xf\n"
      "v_add_f32_dpp v166, v166, v166 row_bcast:31 row_mask:0xc bank_mask:0xf\n"
      "v_mul_f32 v164, 0x3a800000, v164\n"
      "v_mul_f32 v166, 0x3a800000, v166\n"
      "s_nop 0\n"
      "v_readlane_b32 s72, v164, 63\n"
      "v_readlane_b32 s74, v166, 63\n"
      "s_nop 1\n"
      "v_pk_add_f32 v[96:97], v[96:97], s[72:73] op_sel_hi:[1,0] neg_lo:[0,1] neg_hi:[0,1]\n"
      "v_pk_add_f32 v[112:113], v[112:113], s[74:75] op_sel_hi:[1,0] neg_lo:[0,1] neg_hi:[0,1]\n"
      "v_pk_add_f32 v[98:99], v[98:99], s[72:73] op_sel_hi:[1,0] neg_lo:[0,1] neg_hi:[0,1]\n"
      "v_pk_add_f32 v[114:115], v[114:115], s[74:75] op_sel_hi:[1,0] neg_lo:[0,1] neg_hi:[0,1]\n"
      "v_pk_add_f32 v[100:101], v[100:101], s[72:73] op_sel_hi:[1,0] neg_lo:[0,1] neg_hi:[0,1]\n"
      "v_pk_add_f32 v[116:117], v[116:117], s[74:75] op_sel_hi:[1,0] neg_lo:[0,1] neg_hi:[0,1]\n"
      "v_pk_add_f32 v[102:103], v[102:103], s[72:73] op_sel_hi:[1,0] neg_lo:[0,1] neg_hi:[0,1]\n"
      "v_pk_add_f32 v[118:119], v[118:119], s[74:75] op_sel_hi:[1,0] neg_lo:[0,1] neg_hi:[0,1]\n"
      "v_pk_add_f32 v[104:105], v[104:105], s[72:73] op_sel_hi:[1,0] neg_lo:[0,1] neg_hi:[0,1]\n"
      "v_pk_add_f32 v[120:121], v[120:121], s[74:75] op_sel_hi:[1,0] neg_lo:[0,1] neg_hi:[0,1]\n"
      "v_pk_add_f32 v[106:107], v[106:107], s[72:73] op_sel_hi:[1,0] neg_lo:[0,1] neg_hi:[0,1]\n"
      "v_pk_add_f32 v[122:123], v[122:123], s[74:75] op_sel_hi:[1,0] neg_lo:[0,1] neg_hi:[0,1]\n"
      "v_pk_add_f32 v[108:109], v[108:109], s[72:73] op_sel_hi:[1,0] neg_lo:[0,1] neg_hi:[0,1]\n"
      "v_pk_add_f32 v[124:125], v[124:125], s[74:75] op_sel_hi:[1,0] neg_lo:[0,1] neg_hi:[0,1]\n"
      "v_pk_add_f32 v[110:111], v[110:111], s[72:73] op_sel_hi:[1,0] neg_lo:[0,1] neg_hi:[0,1]\n"
      "v_pk_add_f32 v[126:127], v[126:127], s[74:75] op_sel_hi:[1,0] neg_lo:[0,1] neg_hi:[0,1]\n"
      "v_pk_mul_f32 v[164:165], v[96:97], v[96:97]\n"
      "v_pk_mul_f32 v[166:167], v[112:113], v[112:113]\n"
      "v_pk_fma_f32 v[164:165], v[98:99], v[98:99], v[164:165]\n"
      "v_pk_fma_f32 v[166:167], v[114:115], v[114:115], v[166:167]\n"
      "v_pk_fma_f32 v[164:165], v[100:101], v[100:101], v[164:165]\n"
      "v_pk_fma_f32 v[166:167], v[116:117], v[116:117], v[166:167]\n"
      "v_pk_fma_f32 v[164:165], v[102:103], v[102:103], v[164:165]\n"
      "v_pk_fma_f32 v[166:167], v[118:119], v[118:119], v[166:167]\n"
      "v_pk_fma_f32 v[164:165], v[104:105], v[104:105], v[164:165]\n"
      "v_pk_fma_f32 v[166:167], v[120:121], v[120:121], v[166:167]\n"
      "v_pk_fma_f32 v[164:165], v[106:107], v[106:107], v[164:165]\n"
      "v_pk_fma_f32 v[166:167], v[122:123], v[122:123], v[166:167]\n"
      "v_pk_fma_f32 v[164:165], v[108:109], v[108:109], v[164:165]\n"
      "v_pk_fma_f32 v[166:167], v[124:125], v[124:125], v[166:167]\n"
      "v_pk_fma_f32 v[164:165], v[110:111], v[110:111], v[164:165]\n"
      "v_pk_fma_f32 v[166:167], v[126:127], v[126:127], v[166:167]\n"
      "v_add_f32 v164, v164, v165\n"
      "v_add_f32 v166, v166, v167\n"
      "s_nop 0\n"
      "v_add_f32_dpp v164, v164, v164 row_shr:1 row_mask:0xf bank_mask:0xf bound_ctrl:0\n"
      "v_add_f32_dpp v166, v166, v166 row_shr:1 row_mask:0xf bank_mask:0xf bound_ctrl:0\n"
      "s_nop 0\n"
      "v_add_f32_dpp v164, v164, v164 row_shr:2 row_mask:0xf bank_mask:0xf bound_ctrl:0\n"
      "v_add_f32_dpp v166, v166, v166 row_shr:2 row_mask:0xf bank_mask:0xf bound_ctrl:0\n"
      "s_nop 0\n"
      "v_add_f32_dpp v164, v164, v164 row_shr:4 row_mask:0xf bank_mask:0xf bound_ctrl:0\n"
      "v_add_f32_dpp v166, v166, v166 row_shr:4 row_mask:0xf bank_mask:0xf bound_ctrl:0\n"
      "s_nop 0\n"
      "v_add_f32_dpp v164, v164, v164 row_shr:8 row_mask:0xf bank_mask:0xf bound_ctrl:0\n"
      "v_add_f32_dpp v166, v166, v166 row_shr:8 row_mask:0xf bank_mask:0xf bound_ctrl:0\n"
      "s_nop 0\n"
      "v_add_f32_dpp v164, v164, v164 row_bcast:15 row_mask:0xa bank_mask:0xf\n"
      "v_add_f32_dpp v166, v166, v166 row_bcast:15 row_mask:0xa bank_mask:0xf\n"
      "s_nop 0\n"
      "v_add_f32_dpp v164, v164, v164 row_bcast:31 row_mask:0xc bank_mask:0xf\n"
      "v_add_f32_dpp v166, v166, v166 row_bcast:31 row_mask:0xc bank_mask:0xf\n"
      "v_mov_b32 v165, 0x3a800000\n"
      "v_fmaak_f32 v164, v164, v165, 0x3727c5ac\n"
      "v_fmaak_f32 v166, v166, v165, 0x3727c5ac\n"
      "v_rsq_f32 v164, v164\n"
      "v_rsq_f32 v166, v166\n"
      "s_nop 0\n"
      "v_readlane_b32 s72, v164, 63\n"
      "v_readlane_b32 s74, v166, 63\n"
      "s_nop 1\n"
      "s_mov_b32 s80, 0x100000\n"
      "v_pk_mul_f32 v[96:97], v[96:97], s[72:73] op_sel_hi:[1,0]\n"
      "v_pk_mul_f32 v[98:99], v[98:99], s[72:73] op_sel_hi:[1,0]\n"
      "v_pk_mul_f32 v[100:101], v[100:101], s[72:73] op_sel_hi:[1,0]\n"
      "v_pk_mul_f32 v[102:103], v[102:103], s[72:73] op_sel_hi:[1,0]\n"
      "v_pk_mul_f32 v[104:105], v[104:105], s[72:73] op_sel_hi:[1,0]\n"
      "v_pk_mul_f32 v[106:107], v[106:107], s[72:73] op_sel_hi:[1,0]\n"
      "v_pk_mul_f32 v[108:109], v[108:109], s[72:73] op_sel_hi:[1,0]\n"
      "v_pk_mul_f32 v[110:111], v[110:111], s[72:73] op_sel_hi:[1,0]\n"
      "v_cvt_pk_bf16_f32 v64, v96, v97\n"
      "v_cvt_pk_bf16_f32 v65, v98, v99\n"
      "v_cvt_pk_bf16_f32 v66, v100, v101\n"
      "v_cvt_pk_bf16_f32 v67, v102, v103\n"
      "buffer_store_dwordx4 v[64:67], v160, s[64:67], s80 offen offset:0 sc1\n"
      "v_cvt_pk_bf16_f32 v68, v104, v105\n"
      "v_cvt_pk_bf16_f32 v69, v106, v107\n"
      "v_cvt_pk_bf16_f32 v70, v108, v109\n"
      "v_cvt_pk_bf16_f32 v71, v110, v111\n"
      "buffer_store_dwordx4 v[68:71], v160, s[64:67], s80 offen offset:1024 sc1\n"
      "v_pk_mul_f32 v[112:113], v[112:113], s[74:75] op_sel_hi:[1,0]\n"
      "v_pk_mul_f32 v[114:115], v[114:115], s[74:75] op_sel_hi:[1,0]\n"
      "v_pk_mul_f32 v[116:117], v[116:117], s[74:75] op_sel_hi:[1,0]\n"
      "v_pk_mul_f32 v[118:119], v[118:119], s[74:75] op_sel_hi:[1,0]\n"
      "v_pk_mul_f32 v[120:121], v[120:121], s[74:75] op_sel_hi:[1,0]\n"
      "v_pk_mul_f32 v[122:123], v[122:123], s[74:75] op_sel_hi:[1,0]\n"
      "v_pk_mul_f32 v[124:125], v[124:125], s[74:75] op_sel_hi:[1,0]\n"
      "v_pk_mul_f32 v[126:127], v[126:127], s[74:75] op_sel_hi:[1,0]\n"
      "v_cvt_pk_bf16_f32 v72, v112, v113\n"
      "v_cvt_pk_bf16_f32 v73, v114, v115\n"
      "v_cvt_pk_bf16_f32 v74, v116, v117\n"
      "v_cvt_pk_bf16_f32 v75, v118, v119\n"
      "buffer_store_dwordx4 v[72:75], v160, s[64:67], s80 offen offset:2048 sc1\n"
      "v_cvt_pk_bf16_f32 v76, v120, v121\n"
      "v_cvt_pk_bf16_f32 v77, v122, v123\n"
      "v_cvt_pk_bf16_f32 v78, v124, v125\n"
      "v_cvt_pk_bf16_f32 v79, v126, v127\n"
      "buffer_store_dwordx4 v[76:79], v160, s[64:67], s80 offen offset:3072 sc1\n"
      "s_waitcnt vmcnt(5)\n"
      "v_readfirstlane_b32 s54, v162\n"
      "v_readfirstlane_b32 s55, v163\n"
      "s_nop 3\n"
      "s_add_u32 s54, s54, 1\n"
      "s_addc_u32 s55, s55, 0\n"
      "v_and_b32 v200, 15, v4\n"
      "v_lshrrev_b32 v201, 4, v4\n"
      "v_bfe_u32 v202, v4, 2, 2\n"
      "v_and_b32 v203, 3, v4\n"
      "v_and_b32 v204, 7, v4\n"
      "v_lshrrev_b32 v205, 3, v4\n"
      "v_xor_b32 v212, v204, v201\n"
      "v_lshlrev_b32 v212, 4, v212\n"
      "v_xor_b32 v213, 64, v212\n"
      "s_cmp_lt_u32 s56, 2\n"
      "s_cbranch_scc1 Lk_pre0_d\n"
      "s_mul_i32 s72, s61, 448\n"
      "v_add_u32 v206, s72, v4\n"
      "s_movk_i32 s73, 7168\n"
      "v_add_u32 v207, 0, v206\n"
      "v_mul_u32_u24 v208, 2341, v207\n"
      "v_lshrrev_b32 v208, 16, v208\n"
      "v_mul_u32_u24 v209, 28, v208\n"
      "v_sub_u32 v209, v207, v209\n"
      "v_bfe_u32 v210, v208, 3, 1\n"
      "v_lshlrev_b32 v210, 1, v210\n"
      "v_xor_b32 v209, v209, v210\n"
      "v_lshlrev_b32 v209, 4, v209\n"
      "v_mad_u32_u24 v16, v208, s73, v209\n"
      "v_add_u32 v207, 64, v206\n"
      "v_mul_u32_u24 v208, 2341, v207\n"
      "v_lshrrev_b32 v208, 16, v208\n"
      "v_mul_u32_u24 v209, 28, v208\n"
      "v_sub_u32 v209, v207, v209\n"
      "v_bfe_u32 v210, v208, 3, 1\n"
      "v_lshlrev_b32 v210, 1, v210\n"
      "v_xor_b32 v209, v209, v210\n"
      "v_lshlrev_b32 v209, 4, v209\n"
      "v_mad_u32_u24 v17, v208, s73, v209\n"
      "v_subrev_u32 v17, 1024, v17\n"
      "v_add_u32 v207, 128, v206\n"
      "v_mul_u32_u24 v208, 2341, v207\n"
      "v_lshrrev_b32 v208, 16, v208\n"
      "v_mul_u32_u24 v209, 28, v208\n"
      "v_sub_u32 v209, v207, v209\n"
      "v_bfe_u32 v210, v208, 3, 1\n"
      "v_lshlrev_b32 v210, 1, v210\n"
      "v_xor_b32 v209, v209, v210\n"
      "v_lshlrev_b32 v209, 4, v209\n"
      "v_mad_u32_u24 v18, v208, s73, v209\n"
      "v_subrev_u32 v18, 2048, v18\n"
      "v_add_u32 v207, 192, v206\n"
      "v_mul_u32_u24 v208, 2341, v207\n"
      "v_lshrrev_b32 v208, 16, v208\n"
      "v_mul_u32_u24 v209, 28, v208\n"
      "v_sub_u32 v209, v207, v209\n"
      "v_bfe_u32 v210, v208, 3, 1\n"
      "v_lshlrev_b32 v210, 1, v210\n"
      "v_xor_b32 v209, v209, v210\n"
      "v_lshlrev_b32 v209, 4, v209\n"
      "v_mad_u32_u24 v19, v208, s73, v209\n"
      "v_subrev_u32 v19, 3072, v19\n"
      "v_add_u32 v207, 256, v206\n"
      "v_mul_u32_u24 v208, 2341, v207\n"
      "v_lshrrev_b32 v208, 16, v208\n"
      "v_mul_u32_u24 v209, 28, v208\n"
      "v_sub_u32 v209, v207, v209\n"
      "v_bfe_u32 v210, v208, 3, 1\n"
      "v_lshlrev_b32 v210, 1, v210\n"
      "v_xor_b32 v209, v209, v210\n"
      "v_lshlrev_b32 v209, 4, v209\n"
      "v_mad_u32_u24 v20, v208, s73, v209\n"
      "v_add_u32 v207, 320, v206\n"
      "v_mul_u32_u24 v208, 2341, v207\n"
      "v_lshrrev_b32 v208, 16, v208\n"
      "v_mul_u32_u24 v209, 28, v208\n"
      "v_sub_u32 v209, v207, v209\n"
      "v_bfe_u32 v210, v208, 3, 1\n"
      "v_lshlrev_b32 v210, 1, v210\n"
      "v_xor_b32 v209, v209, v210\n"
      "v_lshlrev_b32 v209, 4, v209\n"
      "v_mad_u32_u24 v21, v208, s73, v209\n"
      "v_subrev_u32 v21, 1024, v21\n"
      "v_add_u32 v207, 384, v206\n"
      "v_mul_u32_u24 v208, 2341, v207\n"
      "v_lshrrev_b32 v208, 16, v208\n"
      "v_mul_u32_u24 v209, 28, v208\n"
      "v_sub_u32 v209, v207, v209\n"
      "v_bfe_u32 v210, v208, 3, 1\n"
      "v_lshlrev_b32 v210, 1, v210\n"
      "v_xor_b32 v209, v209, v210\n"
      "v_lshlrev_b32 v209, 4, v209\n"
      "v_mad_u32_u24 v22, v208, s73, v209\n"
      "v_subrev_u32 v22, 2048, v22\n"
      "s_mov_b32 s20, s46\n"
      "s_and_b32 s21, s47, 0xffff\n"
      "s_mov_b32 s22, 0x700000\n"
      "s_mov_b32 s23, 0x20000\n"
      "s_mov_b64 s[24:25], s[64:65]\n"
      "s_mov_b64 s[26:27], s[66:67]\n"
      "s_mul_i32 s81, s60, 448\n"
      "s_lshl_b32 s82, s59, 19\n"
      "s_add_u32 s82, s82, 0x100000\n"
      "s_mov_b32 s30, 0x70000\n"
      "s_waitcnt lgkmcnt(0)\n"
      "s_lshr_b64 s[10:11], s[12:13], 0\n"
      "s_and_b32 s83, s10, 15\n"
      "s_lshr_b64 s[10:11], s[10:11], 4\n"
      "s_mul_i32 s28, s83, s30\n"
      "s_add_u32 s28, s28, s81\n"
      "s_lshl_b32 s29, s83, 7\n"
      "s_add_u32 s29, s29, s82\n"
      "s_branch Lk_pre0_j\n"
      "Lk_pre0_d:\n"
      "s_lshl_b32 s72, s62, 6\n"
      "v_add_u32 v206, s72, v205\n"
      "v_lshlrev_b32 v207, 11, v206\n"
      "v_add_u32 v209, v207, v212\n"
      "v_add_u32 v210, v207, v213\n"
      "v_add_u32 v37, 0x0, v209\n"
      "v_add_u32 v38, 0x3c00, v210\n"
      "v_add_u32 v39, 0x7800, v209\n"
      "v_add_u32 v40, 0xb400, v210\n"
      "v_add_u32 v41, 0x10000, v209\n"
      "v_add_u32 v42, 0x13c00, v210\n"
      "v_add_u32 v43, 0x17800, v209\n"
      "v_add_u32 v44, 0x1b400, v210\n"
      "s_cmp_eq_u32 s63, 0\n"
      "s_cselect_b64 s[24:25], s[42:43], s[44:45]\n"
      "s_and_b32 s25, s25, 0xffff\n"
      "s_mov_b32 s26, 0x200000\n"
      "s_mov_b32 s27, 0x20000\n"
      "s_lshl_b32 s81, s5, 18\n"
      "s_add_u32 s81, s81, 0x100000\n"
      "s_lshl_b32 s82, s58, 18\n"
      "s_movk_i32 s30, 128\n"
      "s_waitcnt lgkmcnt(0)\n"
      "s_lshr_b64 s[10:11], s[12:13], 0\n"
      "s_and_b32 s83, s10, 15\n"
      "s_lshr_b64 s[10:11], s[10:11], 4\n"
      "s_mul_i32 s28, s83, s30\n"
      "s_add_u32 s28, s28, s81\n"
      "s_lshl_b32 s29, s83, 7\n"
      "s_add_u32 s29, s29, s82\n"
      "Lk_pre0_j:\n"
      "s_lshl_b32 s72, s87, 7\n"
      "s_add_u32 s72, s72, 131072\n"
      "s_waitcnt vmcnt(0)\n"
      "s_barrier\n"
      "s_cmp_eq_u32 s61, 0\n"
      "s_cbranch_scc0 Lk_pub_1\n"
      "v_mov_b32 v6, s54\n"
      "v_mov_b32 v7, s55\n"
      "v_min_u32 v5, 1, v4\n"
      "v_lshlrev_b32 v5, 30, v5\n"
      "buffer_store_dwordx2 v[6:7], v5, s[64:67], s72 offen sc1\n"
      "Lk_pub_1:\n"
      "s_cmp_lt_u32 s56, 2\n"
      "s_cbranch_scc1 Lk_w0_d\n"
      "s_add_u32 m0, s86, 73728\n"
      "s_nop 0\n"
      "buffer_load_dwordx4 v16, s[20:23], s28 offen offset:0 lds\n"
      "buffer_load_dwordx4 v17, s[20:23], s28 offen offset:1024 lds\n"
      "buffer_load_dwordx4 v18, s[20:23], s28 offen offset:2048 lds\n"
      "buffer_load_dwordx4 v19, s[20:23], s28 offen offset:3072 lds\n"
      "s_add_u32 m0, s86, 77824\n"
      "s_nop 0\n"
      "buffer_load_dwordx4 v20, s[20:23], s28 offen offset:0 lds\n"
      "buffer_load_dwordx4 v21, s[20:23], s28 offen offset:1024 lds\n"
      "buffer_load_dwordx4 v22, s[20:23], s28 offen offset:2048 lds\n"
      "s_branch Lk_w0_j\n"
      "Lk_w0_d:\n"
      "s_add_u32 m0, s85, 0\n"
      "s_nop 0\n"
      "buffer_load_dwordx4 v37, s[24:27], s29 offen offset:0 lds\n"
      "buffer_load_dwordx4 v38, s[24:27], s29 offen offset:1024 lds\n"
      "buffer_load_dwordx4 v39, s[24:27], s29 offen offset:2048 lds\n"
      "buffer_load_dwordx4 v40, s[24:27], s29 offen offset:3072 lds\n"
      "s_add_u32 m0, s85, 4096\n"
      "s_nop 0\n"
      "buffer_load_dwordx4 v41, s[24:27], s29 offen offset:0 lds\n"
      "buffer_load_dwordx4 v42, s[24:27], s29 offen offset:1024 lds\n"
      "buffer_load_dwordx4 v43, s[24:27], s29 offen offset:2048 lds\n"
      "buffer_load_dwordx4 v44, s[24:27], s29 offen offset:3072 lds\n"
      "Lk_w0_j:\n"
      "s_lshl_b32 s76, s61, 1\n"
      "s_xor_b32 s76, s76, s56\n"
      "s_xor_b32 s77, s76, 1\n"
      "s_lshl_b32 s72, s76, 5\n"
      "v_add_u32 v206, s72, v205\n"
      "v_lshlrev_b32 v207, 12, v206\n"
      "v_add_u32 v209, v207, v212\n"
      "v_add_u32 v210, v207, v213\n"
      "v_add_u32 v208, 0x0, v209\n"
      "v_accvgpr_write_b32 a208, v208\n"
      "v_add_u32 v208, 0x7c00, v210\n"
      "v_accvgpr_write_b32 a209, v208\n"
      "v_add_u32 v208, 0xf800, v209\n"
      "v_accvgpr_write_b32 a210, v208\n"
      "v_add_u32 v208, 0x17400, v210\n"
      "v_accvgpr_write_b32 a211, v208\n"
      "s_lshl_b32 s72, s77, 5\n"
      "v_add_u32 v206, s72, v205\n"
      "v_lshlrev_b32 v207, 12, v206\n"
      "v_add_u32 v209, v207, v212\n"
      "v_add_u32 v210, v207, v213\n"
      "v_add_u32 v208, 0x0, v209\n"
      "v_accvgpr_write_b32 a212, v208\n"
      "v_add_u32 v208, 0x7c00, v210\n"
      "v_accvgpr_write_b32 a213, v208\n"
      "v_add_u32 v208, 0xf800, v209\n"
      "v_accvgpr_write_b32 a214, v208\n"
      "v_add_u32 v208, 0x17400, v210\n"
      "v_accvgpr_write_b32 a215, v208\n"
      "v_lshlrev_b32 v211, 4, v4\n"
      "v_add_u32 v207, s92, v211\n"
      "v_accvgpr_write_b32 a220, v207\n"
      "s_lshl_b32 s72, s76, 12\n"
      "v_add_u32 v207, s72, v211\n"
      "v_accvgpr_write_b32 a221, v207\n"
      "s_lshl_b32 s72, s77, 12\n"
      "v_add_u32 v207, s72, v211\n"
      "v_accvgpr_write_b32 a222, v207\n"
      "s_lshl_b32 s72, s61, 4\n"
      "v_add_u32 v206, s72, v201\n"
      "v_lshlrev_b32 v207, 11, v206\n"
      "v_lshlrev_b32 v208, 1, v201\n"
      "v_xor_b32 v208, v200, v208\n"
      "v_lshlrev_b32 v208, 4, v208\n"
      "v_xor_b32 v209, 128, v208\n"
      "v_add_u32 v210, v207, v208\n"
      "v_add_u32 v1, 0x0, v210\n"
      "v_add_u32 v210, v207, v208\n"
      "v_add_u32 v2, 0x1c00, v210\n"
      "v_add_u32 v210, v207, v209\n"
      "v_add_u32 v3, 0x3800, v210\n"
      "v_add_u32 v210, v207, v209\n"
      "v_add_u32 v63, 0x5400, v210\n"
      "v_lshrrev_b32 v206, 1, v200\n"
      "v_xor_b32 v206, v206, v201\n"
      "v_lshlrev_b32 v206, 4, v206\n"
      "v_lshl_add_u32 v214, v200, 7, v206\n"
      "s_lshl_b32 s72, s62, 13\n"
      "s_lshl_b32 s73, s63, 14\n"
      "v_add_u32 v45, s72, v214\n"
      "v_xor_b32 v46, 64, v45\n"
      "v_add_u32 v47, s73, v214\n"
      "v_xor_b32 v48, 64, v47\n"
      "v_accvgpr_write_b32 a225, v47\n"
      "v_accvgpr_write_b32 a226, v48\n"
      "v_add_u32 v207, s72, v211\n"
      "v_accvgpr_write_b32 a223, v207\n"
      "v_add_u32 v207, 1024, v207\n"
      "v_accvgpr_write_b32 a224, v207\n"
      "v_add_u32 v53, s73, v211\n"
      "v_add_u32 v54, 1024, v53\n"
      "v_lshl_add_u32 v206, v201, 2, v202\n"
      "v_and_b32 v207, 7, v206\n"
      "v_lshl_add_u32 v206, v201, 3, v202\n"
      "v_lshlrev_b32 v206, 8, v206\n"
      "v_lshl_add_u32 v206, v203, 3, v206\n"
      "s_lshl_b32 s75, s62, 2\n"
      "s_add_u32 s76, s75, 0\n"
      "v_xor_b32 v208, s76, v207\n"
      "v_lshl_add_u32 v49, v208, 5, v206\n"
      "s_add_u32 s76, s75, 1\n"
      "v_xor_b32 v208, s76, v207\n"
      "v_lshl_add_u32 v50, v208, 5, v206\n"
      "s_add_u32 s76, s75, 2\n"
      "v_xor_b32 v208, s76, v207\n"
      "v_lshl_add_u32 v51, v208, 5, v206\n"
      "s_add_u32 s76, s75, 3\n"
      "v_xor_b32 v208, s76, v207\n"
      "v_lshl_add_u32 v52, v208, 5, v206\n"
      "s_cmp_lt_u32 s56, 2\n"
      "s_cbranch_scc1 Lk_prog_d\n"
      "Lk_prog_h:\n"
      "s_lshl_b32 s72, s61, 6\n"
      "v_add_u32 v206, s72, v205\n"
      "v_lshlrev_b32 v207, 11, v206\n"
      "v_add_u32 v209, v207, v212\n"
      "v_add_u32 v210, v207, v213\n"
      "v_add_u32 v8, 0x0, v209\n"
      "v_add_u32 v9, 0x3c00, v210\n"
      "v_add_u32 v10, 0x7800, v209\n"
      "v_add_u32 v11, 0xb400, v210\n"
      "v_add_u32 v12, 0x10000, v209\n"
      "v_add_u32 v13, 0x13c00, v210\n"
      "v_add_u32 v14, 0x17800, v209\n"
      "v_add_u32 v15, 0x1b400, v210\n"
      "v_add_u32 v25, 102400, v47\n"
      "v_add_u32 v26, 102400, v48\n"
      "v_lshl_add_u32 v206, v201, 3, v202\n"
      "v_mul_u32_u24 v206, 448, v206\n"
      "v_lshl_add_u32 v206, v203, 3, v206\n"
      "v_and_b32 v207, 1, v201\n"
      "v_lshlrev_b32 v208, 5, v207\n"
      "s_lshl_b32 s75, s62, 8\n"
      "v_add_u32 v209, s75, v206\n"
      "v_add_u32 v27, v209, v208\n"
      "v_sub_u32 v28, v209, v208\n"
      "s_add_u32 s76, s62, 6\n"
      "v_xor_b32 v208, s76, v207\n"
      "v_lshl_add_u32 v29, v208, 5, v206\n"
      "v_add_u32 v30, 135168, v27\n"
      "v_add_u32 v27, 73728, v27\n"
      "v_add_u32 v31, 135168, v28\n"
      "v_add_u32 v28, 73728, v28\n"
      "v_add_u32 v32, 135168, v29\n"
      "v_add_u32 v29, 73728, v29\n"
      "s_and_b32 s83, s56, 1\n"
      "s_mul_i32 s83, s83, 15\n"
      "s_lshl_b32 s91, s83, 17\n"
      "s_lshl_b32 s98, s58, 8\n"
      "s_add_u32 s91, s91, s98\n"
      "s_mul_i32 s98, s57, 56\n"
      "s_add_u32 s98, s98, s83\n"
      "s_lshl_b32 s98, s98, 15\n"
      "s_add_u32 s98, s98, 0x1000000\n"
      "s_lshl_b32 s80, s59, 12\n"
      "s_add_u32 s80, s80, 131072\n"
      "v_and_b32 v55, 31, v4\n"
      "v_lshlrev_b32 v55, 7, v55\n"
      "v_accvgpr_read_b32 v33, a220\n"
      "v_accvgpr_read_b32 v34, a220\n"
      "v_accvgpr_read_b32 v35, a220\n"
      "v_accvgpr_read_b32 v36, a220\n"
      "v_accvgpr_read_b32 v37, a208\n"
      "v_accvgpr_read_b32 v38, a209\n"
      "v_accvgpr_read_b32 v39, a210\n"
      "v_accvgpr_read_b32 v40, a211\n"
      "v_accvgpr_read_b32 v41, a212\n"
      "v_accvgpr_read_b32 v42, a213\n"
      "v_accvgpr_read_b32 v43, a214\n"
      "v_accvgpr_read_b32 v44, a215\n"
      "v_accvgpr_read_b32 v45, a223\n"
      "v_accvgpr_read_b32 v46, a224\n"
      "v_accvgpr_read_b32 v47, a225\n"
      "v_accvgpr_read_b32 v48, a226\n"
      "s_mov_b32 s94, 0\n"
      "s_mov_b32 s99, 49152\n"
      "s_and_b32 s83, s56, 1\n"
      "s_mul_i32 s83, s83, 9\n"
      "s_add_u32 s83, s83, 14\n"
      "s_lshl_b32 s78, s58, 6\n"
      "s_add_u32 s78, s78, s83\n"
      "s_lshl_b32 s78, s78, 14\n"
      "s_add_u32 s78, s78, 0x880000\n"
      "s_lshl_b32 s79, s57, 20\n"
      "s_lshl_b32 s83, s83, 7\n"
      "s_add_u32 s79, s79, s83\n"
      "s_add_u32 s72, s85, 4096\n"
      "s_mov_b32 s95, 102400\n"
      "s_mov_b32 s96, 0\n"
      "s_mov_b32 s97, 40960\n"
      "s_cmp_eq_u32 s61, 0\n"
      "s_cbranch_scc0 Lk_pw_2\n"
      "s_mov_b32 s84, 0x40000\n"
      "Lk_pl_4:\n"
      "buffer_load_dwordx2 v[56:57], v55, s[64:67], s80 offen sc1\n"
      "s_waitcnt vmcnt(0)\n"
      "v_cmp_ne_u64 vcc, s[54:55], v[56:57]\n"
      "s_cbranch_vccz Lk_pd_3\n"
      "s_sleep 1\n"
      "s_sub_u32 s84, s84, 1\n"
      "s_cmp_lg_u32 s84, 0\n"
      "s_cbranch_scc1 Lk_pl_4\n"
      "Lk_pd_3:\n"
      "Lk_pw_2:\n"
      "s_barrier\n"
      "s_add_u32 m0, s85, 102400\n"
      "s_nop 0\n"
      "buffer_load_dwordx4 v8, s[24:27], s29 offen offset:0 sc1 lds\n"
      "buffer_load_dwordx4 v9, s[24:27], s29 offen offset:1024 sc1 lds\n"
      "buffer_load_dwordx4 v10, s[24:27], s29 offen offset:2048 sc1 lds\n"
      "buffer_load_dwordx4 v11, s[24:27], s29 offen offset:3072 sc1 lds\n"
      "s_add_u32 m0, s85, 106496\n"
      "s_nop 0\n"
      "buffer_load_dwordx4 v12, s[24:27], s29 offen offset:0 sc1 lds\n"
      "buffer_load_dwordx4 v13, s[24:27], s29 offen offset:1024 sc1 lds\n"
      "buffer_load_dwordx4 v14, s[24:27], s29 offen offset:2048 sc1 lds\n"
      "buffer_load_dwordx4 v15, s[24:27], s29 offen offset:3072 sc1 lds\n"
      "s_and_b32 s83, s10, 15\n"
      "s_lshr_b64 s[10:11], s[10:11], 4\n"
      "s_mul_i32 s28, s83, s30\n"
      "s_add_u32 s28, s28, s81\n"
      "s_lshl_b32 s29, s83, 7\n"
      "s_add_u32 s29, s29, s82\n"
      "s_add_u32 m0, s86, 135168\n"
      "s_nop 0\n"
      "buffer_load_dwordx4 v16, s[20:23], s28 offen offset:0 lds\n"
      "buffer_load_dwordx4 v17, s[20:23], s28 offen offset:1024 lds\n"
      "buffer_load_dwordx4 v18, s[20:23], s28 offen offset:2048 lds\n"
      "buffer_load_dwordx4 v19, s[20:23], s28 offen offset:3072 lds\n"
      "s_add_u32 m0, s86, 139264\n"
      "s_nop 0\n"
      "buffer_load_dwordx4 v20, s[20:23], s28 offen offset:0 lds\n"
      "buffer_load_dwordx4 v21, s[20:23], s28 offen offset:1024 lds\n"
      "buffer_load_dwordx4 v22, s[20:23], s28 offen offset:2048 lds\n"
      "s_lshl_b32 s80, s58, 11\n"
      "s_add_u32 s80, s80, 196608\n"
      "v_and_b32 v55, 15, v4\n"
      "v_lshlrev_b32 v55, 7, v55\n"
      "s_waitcnt vmcnt(7)\n"
      "s_barrier\n"
      "ds_read_b64_tr_b16 a[0:1], v27 offset:0\n"
      "ds_read_b64_tr_b16 a[2:3], v27 offset:1792\n"
      "ds_read_b128 a[28:31], v25 offset:0\n"
      "ds_read_b128 a[32:35], v25 offset:2048\n"
      "ds_read_b128 a[36:39], v25 offset:4096\n"
      "ds_read_b128 a[40:43], v25 offset:6144\n"
      "ds_read_b128 a[44:47], v25 offset:8192\n"
      "ds_read_b128 a[48:51], v25 offset:10240\n"
      "ds_read_b128 a[52:55], v25 offset:12288\n"
      "ds_read_b128 a[56:59], v25 offset:14336\n"
      "ds_read_b64_tr_b16 a[4:5], v28 offset:32\n"
      "ds_read_b64_tr_b16 a[6:7], v28 offset:1824\n"
      "ds_read_b64_tr_b16 a[8:9], v27 offset:64\n"
      "ds_read_b64_tr_b16 a[10:11], v27 offset:1856\n"
      "ds_read_b64_tr_b16 a[12:13], v28 offset:96\n"
      "ds_read_b64_tr_b16 a[14:15], v28 offset:1888\n"
      "ds_read_b64_tr_b16 a[16:17], v27 offset:128\n"
      "ds_read_b64_tr_b16 a[18:19], v27 offset:1920\n"
      "ds_read_b64_tr_b16 a[20:21], v28 offset:160\n"
      "ds_read_b64_tr_b16 a[22:23], v28 offset:1952\n"
      "ds_read_b64_tr_b16 a[24:25], v29 offset:0\n"
      "ds_read_b64_tr_b16 a[26:27], v29 offset:1792\n"
      "s_waitcnt lgkmcnt(0)\n"
      "v_mfma_f32_16x16x32_bf16 v[64:67], a[0:3], a[28:31], 0\n"
      "ds_read_b64_tr_b16 a[60:61], v27 offset:14336\n"
      "s_add_u32 m0, s85, 0\n"
      "v_mfma_f32_16x16x32_bf16 v[68:71], a[0:3], a[32:35], 0\n"
      "buffer_load_dwordx4 v8, s[24:27], s29 offen offset:0 sc1 lds\n"
      "v_mfma_f32_16x16x32_bf16 v[72:75], a[0:3], a[36:39], 0\n"
      "ds_read_b64_tr_b16 a[62:63], v27 offset:16128\n"
      "v_mfma_f32_16x16x32_bf16 v[76:79], a[0:3], a[40:43], 0\n"
      "buffer_load_dwordx4 v9, s[24:27], s29 offen offset:1024 sc1 lds\n"
      "v_mfma_f32_16x16x32_bf16 v[80:83], a[0:3], a[44:47], 0\n"
      "ds_read_b128 a[88:91], v26 offset:0\n"
      "v_mfma_f32_16x16x32_bf16 v[84:87], a[0:3], a[48:51], 0\n"
      "v_mfma_f32_16x16x32_bf16 v[88:91], a[0:3], a[52:55], 0\n"
      "ds_read_b128 a[92:95], v26 offset:2048\n"
      "buffer_load_dwordx4 v10, s[24:27], s29 offen offset:2048 sc1 lds\n"
      "v_mfma_f32_16x16x32_bf16 v[92:95], a[0:3], a[56:59], 0\n"
      "v_mfma_f32_16x16x32_bf16 v[124:127], a[4:7], a[56:59], 0\n"
      "ds_read_b128 a[96:99], v26 offset:4096\n"
      "v_mfma_f32_16x16x32_bf16 v[120:123], a[4:7], a[52:55], 0\n"
      "buffer_load_dwordx4 v11, s[24:27], s29 offen offset:3072 sc1 lds\n"
      "v_mfma_f32_16x16x32_bf16 v[116:119], a[4:7], a[48:51], 0\n"
      "ds_read_b128 a[100:103], v26 offset:6144\n"
      "v_mfma_f32_16x16x32_bf16 v[112:115], a[4:7], a[44:47], 0\n"
      "s_add_u32 m0, s85, 4096\n"
      "v_mfma_f32_16x16x32_bf16 v[108:111], a[4:7], a[40:43], 0\n"
      "ds_read_b128 a[104:107], v26 offset:8192\n"
      "buffer_load_dwordx4 v12, s[24:27], s29 offen offset:0 sc1 lds\n"
      "v_mfma_f32_16x16x32_bf16 v[104:107], a[4:7], a[36:39], 0\n"
      "v_mfma_f32_16x16x32_bf16 v[100:103], a[4:7], a[32:35], 0\n"
      "ds_read_b128 a[108:111], v26 offset:10240\n"
      "v_mfma_f32_16x16x32_bf16 v[96:99], a[4:7], a[28:31], 0\n"
      "buffer_load_dwordx4 v13, s[24:27], s29 offen offset:1024 sc1 lds\n"
      "v_mfma_f32_16x16x32_bf16 v[128:131], a[8:11], a[28:31], 0\n"
      "ds_read_b128 a[112:115], v26 offset:12288\n"
      "v_mfma_f32_16x16x32_bf16 v[132:135], a[8:11], a[32:35], 0\n"
      "v_mfma_f32_16x16x32_bf16 v[136:139], a[8:11], a[36:39], 0\n"
      "ds_read_b128 a[116:119], v26 offset:14336\n"
      "buffer_load_dwordx4 v14, s[24:27], s29 offen offset:2048 sc1 lds\n"
      "v_mfma_f32_16x16x32_bf16 v[140:143], a[8:11], a[40:43], 0\n"
      "v_mfma_f32_16x16x32_bf16 v[144:147], a[8:11], a[44:47], 0\n"
      "ds_read_b64_tr_b16 a[64:65], v28 offset:14368\n"
      "v_mfma_f32_16x16x32_bf16 v[148:151], a[8:11], a[48:51], 0\n"
      "buffer_load_dwordx4 v15, s[24:27], s29 offen offset:3072 sc1 lds\n"
      "s_and_b32 s83, s10, 15\n"
      "s_lshr_b64 s[10:11], s[10:11], 4\n"
      "s_mul_i32 s28, s83, s30\n"
      "s_add_u32 s28, s28, s81\n"
      "s_lshl_b32 s29, s83, 7\n"
      "s_add_u32 s29, s29, s82\n"
      "v_mfma_f32_16x16x32_bf16 v[152:155], a[8:11], a[52:55], 0\n"
      "ds_read_b64_tr_b16 a[66:67], v28 offset:16160\n"
      "v_mfma_f32_16x16x32_bf16 v[156:159], a[8:11], a[56:59], 0\n"
      "s_add_u32 m0, s85, 40960\n"
      "v_mfma_f32_16x16x32_bf16 v[188:191], a[12:15], a[56:59], 0\n"
      "ds_read_b64_tr_b16 a[68:69], v27 offset:14400\n"
      "buffer_load_dwordx4 v8, s[24:27], s29 offen offset:0 sc1 lds\n"
      "v_mfma_f32_16x16x32_bf16 v[184:187], a[12:15], a[52:55], 0\n"
      "v_mfma_f32_16x16x32_bf16 v[180:183], a[12:15], a[48:51], 0\n"
      "ds_read_b64_tr_b16 a[70:71], v27 offset:16192\n"
      "v_mfma_f32_16x16x32_bf16 v[176:179], a[12:15], a[44:47], 0\n"
      "buffer_load_dwordx4 v9, s[24:27], s29 offen offset:1024 sc1 lds\n"
      "v_mfma_f32_16x16x32_bf16 v[172:175], a[12:15], a[40:43], 0\n"
      "ds_read_b64_tr_b16 a[72:73], v28 offset:14432\n"
      "v_mfma_f32_16x16x32_bf16 v[168:171], a[12:15], a[36:39], 0\n"
      "v_mfma_f32_16x16x32_bf16 v[164:167], a[12:15], a[32:35], 0\n"
      "ds_read_b64_tr_b16 a[74:75], v28 offset:16224\n"
      "buffer_load_dwordx4 v10, s[24:27], s29 offen offset:2048 sc1 lds\n"
      "v_mfma_f32_16x16x32_bf16 v[160:163], a[12:15], a[28:31], 0\n"
      "v_mfma_f32_16x16x32_bf16 v[192:195], a[16:19], a[28:31], 0\n"
      "ds_read_b64_tr_b16 a[76:77], v27 offset:14464\n"
      "v_mfma_f32_16x16x32_bf16 v[196:199], a[16:19], a[32:35], 0\n"
      "buffer_load_dwordx4 v11, s[24:27], s29 offen offset:3072 sc1 lds\n"
      "v_mfma_f32_16x16x32_bf16 v[200:203], a[16:19], a[36:39], 0\n"
      "ds_read_b64_tr_b16 a[78:79], v27 offset:16256\n"
      "v_mfma_f32_16x16x32_bf16 v[204:207], a[16:19], a[40:43], 0\n"
      "s_add_u32 m0, s85, 45056\n"
      "v_mfma_f32_16x16x32_bf16 v[208:211], a[16:19], a[44:47], 0\n"
      "ds_read_b64_tr_b16 a[80:81], v28 offset:14496\n"
      "buffer_load_dwordx4 v12, s[24:27], s29 offen offset:0 sc1 lds\n"
      "v_mfma_f32_16x16x32_bf16 v[212:215], a[16:19], a[48:51], 0\n"
      "v_mfma_f32_16x16x32_bf16 v[216:219], a[16:19], a[52:55], 0\n"
      "ds_read_b64_tr_b16 a[82:83], v28 offset:16288\n"
      "v_mfma_f32_16x16x32_bf16 v[220:223], a[16:19], a[56:59], 0\n"
      "buffer_load_dwordx4 v13, s[24:27], s29 offen offset:1024 sc1 lds\n"
      "v_mfma_f32_16x16x32_bf16 v[252:255], a[20:23], a[56:59], 0\n"
      "ds_read_b64_tr_b16 a[84:85], v29 offset:14336\n"
      "v_mfma_f32_16x16x32_bf16 v[248:251], a[20:23], a[52:55], 0\n"
      "v_mfma_f32_16x16x32_bf16 v[244:247], a[20:23], a[48:51], 0\n"
      "ds_read_b64_tr_b16 a[86:87], v29 offset:16128\n"
      "buffer_load_dwordx4 v14, s[24:27], s29 offen offset:2048 sc1 lds\n"
      "v_mfma_f32_16x16x32_bf16 v[240:243], a[20:23], a[44:47], 0\n"
      "v_mfma_f32_16x16x32_bf16 v[236:239], a[20:23], a[40:43], 0\n"
      "v_mfma_f32_16x16x32_bf16 v[232:235], a[20:23], a[36:39], 0\n"
      "buffer_load_dwordx4 v15, s[24:27], s29 offen offset:3072 sc1 lds\n"
      "v_mfma_f32_16x16x32_bf16 v[228:231], a[20:23], a[32:35], 0\n"
      "v_mfma_f32_16x16x32_bf16 v[224:227], a[20:23], a[28:31], 0\n"
      "v_mfma_f32_16x16x32_bf16 a[128:131], a[24:27], a[28:31], 0\n"
      "s_sub_u32 s31, s96, s95\n"
      "v_mfma_f32_16x16x32_bf16 a[132:135], a[24:27], a[32:35], 0\n"
      "v_add_u32 v25, s31, v25\n"
      "v_mfma_f32_16x16x32_bf16 a[136:139], a[24:27], a[36:39], 0\n"
      "v_add_u32 v26, s31, v26\n"
      "v_mfma_f32_16x16x32_bf16 a[140:143], a[24:27], a[40:43], 0\n"
      "s_mov_b32 s31, s95\n"
      "v_mfma_f32_16x16x32_bf16 a[144:147], a[24:27], a[44:47], 0\n"
      "s_mov_b32 s95, s96\n"
      "v_mfma_f32_16x16x32_bf16 a[148:151], a[24:27], a[48:51], 0\n"
      "s_mov_b32 s96, s97\n"
      "v_mfma_f32_16x16x32_bf16 a[152:155], a[24:27], a[52:55], 0\n"
      "s_mov_b32 s97, s31\n"
      "v_mfma_f32_16x16x32_bf16 a[156:159], a[24:27], a[56:59], 0\n"
      "s_mov_b32 s93, 6\n"
      "Lk_g1h:\n"
      "s_waitcnt vmcnt(8) lgkmcnt(0)\n"
      "s_barrier\n"
      "v_mfma_f32_16x16x32_bf16 v[64:67], a[60:63], a[88:91], v[64:67]\n"
      "ds_read_b64_tr_b16 a[0:1], v30 offset:0\n"
      "v_mfma_f32_16x16x32_bf16 v[68:71], a[60:63], a[92:95], v[68:71]\n"
      "v_mfma_f32_16x16x32_bf16 v[72:75], a[60:63], a[96:99], v[72:75]\n"
      "ds_read_b64_tr_b16 a[2:3], v30 offset:1792\n"
      "s_add_u32 m0, s86, 73728\n"
      "v_mfma_f32_16x16x32_bf16 v[76:79], a[60:63], a[100:103], v[76:79]\n"
      "buffer_load_dwordx4 v16, s[20:23], s28 offen offset:0 lds\n"
      "v_mfma_f32_16x16x32_bf16 v[80:83], a[60:63], a[104:107], v[80:83]\n"
      "ds_read_b128 a[28:31], v25 offset:0\n"
      "v_mfma_f32_16x16x32_bf16 v[84:87], a[60:63], a[108:111], v[84:87]\n"
      "v_mfma_f32_16x16x32_bf16 v[88:91], a[60:63], a[112:115], v[88:91]\n"
      "ds_read_b128 a[32:35], v25 offset:2048\n"
      "v_mfma_f32_16x16x32_bf16 v[92:95], a[60:63], a[116:119], v[92:95]\n"
      "buffer_load_dwordx4 v17, s[20:23], s28 offen offset:1024 lds\n"
      "v_mfma_f32_16x16x32_bf16 v[124:127], a[64:67], a[116:119], v[124:127]\n"
      "ds_read_b128 a[36:39], v25 offset:4096\n"
      "v_mfma_f32_16x16x32_bf16 v[120:123], a[64:67], a[112:115], v[120:123]\n"
      "v_mfma_f32_16x16x32_bf16 v[116:119], a[64:67], a[108:111], v[116:119]\n"
      "ds_read_b128 a[40:43], v25 offset:6144\n"
      "v_mfma_f32_16x16x32_bf16 v[112:115], a[64:67], a[104:107], v[112:115]\n"
      "buffer_load_dwordx4 v18, s[20:23], s28 offen offset:2048 lds\n"
      "v_mfma_f32_16x16x32_bf16 v[108:111], a[64:67], a[100:103], v[108:111]\n"
      "ds_read_b128 a[44:47], v25 offset:8192\n"
      "v_mfma_f32_16x16x32_bf16 v[104:107], a[64:67], a[96:99], v[104:107]\n"
      "v_mfma_f32_16x16x32_bf16 v[100:103], a[64:67], a[92:95], v[100:103]\n"
      "ds_read_b128 a[48:51], v25 offset:10240\n"
      "v_mfma_f32_16x16x32_bf16 v[96:99], a[64:67], a[88:91], v[96:99]\n"
      "buffer_load_dwordx4 v19, s[20:23], s28 offen offset:3072 lds\n"
      "v_mfma_f32_16x16x32_bf16 v[128:131], a[68:71], a[88:91], v[128:131]\n"
      "ds_read_b128 a[52:55], v25 offset:12288\n"
      "v_mfma_f32_16x16x32_bf16 v[132:135], a[68:71], a[92:95], v[132:135]\n"
      "v_mfma_f32_16x16x32_bf16 v[136:139], a[68:71], a[96:99], v[136:139]\n"
      "ds_read_b128 a[56:59], v25 offset:14336\n"
      "s_add_u32 m0, s86, 77824\n"
      "v_mfma_f32_16x16x32_bf16 v[140:143], a[68:71], a[100:103], v[140:143]\n"
      "buffer_load_dwordx4 v20, s[20:23], s28 offen offset:0 lds\n"
      "v_mfma_f32_16x16x32_bf16 v[144:147], a[68:71], a[104:107], v[144:147]\n"
      "ds_read_b64_tr_b16 a[4:5], v31 offset:32\n"
      "v_mfma_f32_16x16x32_bf16 v[148:151], a[68:71], a[108:111], v[148:151]\n"
      "v_mfma_f32_16x16x32_bf16 v[152:155], a[68:71], a[112:115], v[152:155]\n"
      "ds_read_b64_tr_b16 a[6:7], v31 offset:1824\n"
      "v_mfma_f32_16x16x32_bf16 v[156:159], a[68:71], a[116:119], v[156:159]\n"
      "buffer_load_dwordx4 v21, s[20:23], s28 offen offset:1024 lds\n"
      "v_mfma_f32_16x16x32_bf16 v[188:191], a[72:75], a[116:119], v[188:191]\n"
      "ds_read_b64_tr_b16 a[8:9], v30 offset:64\n"
      "v_mfma_f32_16x16x32_bf16 v[184:187], a[72:75], a[112:115], v[184:187]\n"
      "v_mfma_f32_16x16x32_bf16 v[180:183], a[72:75], a[108:111], v[180:183]\n"
      "ds_read_b64_tr_b16 a[10:11], v30 offset:1856\n"
      "v_mfma_f32_16x16x32_bf16 v[176:179], a[72:75], a[104:107], v[176:179]\n"
      "buffer_load_dwordx4 v22, s[20:23], s28 offen offset:2048 lds\n"
      "v_mfma_f32_16x16x32_bf16 v[172:175], a[72:75], a[100:103], v[172:175]\n"
      "ds_read_b64_tr_b16 a[12:13], v31 offset:96\n"
      "v_mfma_f32_16x16x32_bf16 v[168:171], a[72:75], a[96:99], v[168:171]\n"
      "s_and_b32 s83, s10, 15\n"
      "s_lshr_b64 s[10:11], s[10:11], 4\n"
      "v_mfma_f32_16x16x32_bf16 v[164:167], a[72:75], a[92:95], v[164:167]\n"
      "ds_read_b64_tr_b16 a[14:15], v31 offset:1888\n"
      "v_mfma_f32_16x16x32_bf16 v[160:163], a[72:75], a[88:91], v[160:163]\n"
      "s_mul_i32 s28, s83, s30\n"
      "s_add_u32 s28, s28, s81\n"
      "v_mfma_f32_16x16x32_bf16 v[192:195], a[76:79], a[88:91], v[192:195]\n"
      "ds_read_b64_tr_b16 a[16:17], v30 offset:128\n"
      "v_mfma_f32_16x16x32_bf16 v[196:199], a[76:79], a[92:95], v[196:199]\n"
      "s_lshl_b32 s29, s83, 7\n"
      "s_add_u32 s29, s29, s82\n"
      "v_mfma_f32_16x16x32_bf16 v[200:203], a[76:79], a[96:99], v[200:203]\n"
      "ds_read_b64_tr_b16 a[18:19], v30 offset:1920\n"
      "v_mfma_f32_16x16x32_bf16 v[204:207], a[76:79], a[100:103], v[204:207]\n"
      "v_mfma_f32_16x16x32_bf16 v[208:211], a[76:79], a[104:107], v[208:211]\n"
      "ds_read_b64_tr_b16 a[20:21], v31 offset:160\n"
      "v_mfma_f32_16x16x32_bf16 v[212:215], a[76:79], a[108:111], v[212:215]\n"
      "v_mfma_f32_16x16x32_bf16 v[216:219], a[76:79], a[112:115], v[216:219]\n"
      "ds_read_b64_tr_b16 a[22:23], v31 offset:1952\n"
      "v_mfma_f32_16x16x32_bf16 v[220:223], a[76:79], a[116:119], v[220:223]\n"
      "v_mfma_f32_16x16x32_bf16 v[252:255], a[80:83], a[116:119], v[252:255]\n"
      "ds_read_b64_tr_b16 a[24:25], v32 offset:0\n"
      "v_mfma_f32_16x16x32_bf16 v[248:251], a[80:83], a[112:115], v[248:251]\n"
      "v_mfma_f32_16x16x32_bf16 v[244:247], a[80:83], a[108:111], v[244:247]\n"
      "ds_read_b64_tr_b16 a[26:27], v32 offset:1792\n"
      "v_mfma_f32_16x16x32_bf16 v[240:243], a[80:83], a[104:107], v[240:243]\n"
      "v_mfma_f32_16x16x32_bf16 v[236:239], a[80:83], a[100:103], v[236:239]\n"
      "v_mfma_f32_16x16x32_bf16 v[232:235], a[80:83], a[96:99], v[232:235]\n"
      "v_mfma_f32_16x16x32_bf16 v[228:231], a[80:83], a[92:95], v[228:231]\n"
      "v_mfma_f32_16x16x32_bf16 v[224:227], a[80:83], a[88:91], v[224:227]\n"
      "v_mfma_f32_16x16x32_bf16 a[128:131], a[84:87], a[88:91], a[128:131]\n"
      "v_mfma_f32_16x16x32_bf16 a[132:135], a[84:87], a[92:95], a[132:135]\n"
      "v_mfma_f32_16x16x32_bf16 a[136:139], a[84:87], a[96:99], a[136:139]\n"
      "v_mfma_f32_16x16x32_bf16 a[140:143], a[84:87], a[100:103], a[140:143]\n"
      "v_mfma_f32_16x16x32_bf16 a[144:147], a[84:87], a[104:107], a[144:147]\n"
      "v_mfma_f32_16x16x32_bf16 a[148:151], a[84:87], a[108:111], a[148:151]\n"
      "v_mfma_f32_16x16x32_bf16 a[152:155], a[84:87], a[112:115], a[152:155]\n"
      "v_mfma_f32_16x16x32_bf16 a[156:159], a[84:87], a[116:119], a[156:159]\n"
      "s_waitcnt lgkmcnt(0)\n"
      "v_mfma_f32_16x16x32_bf16 v[64:67], a[0:3], a[28:31], v[64:67]\n"
      "ds_read_b64_tr_b16 a[60:61], v30 offset:14336\n"
      "v_mfma_f32_16x16x32_bf16 v[68:71], a[0:3], a[32:35], v[68:71]\n"
      "v_mfma_f32_16x16x32_bf16 v[72:75], a[0:3], a[36:39], v[72:75]\n"
      "ds_read_b64_tr_b16 a[62:63], v30 offset:16128\n"
      "v_mfma_f32_16x16x32_bf16 v[76:79], a[0:3], a[40:43], v[76:79]\n"
      "v_mfma_f32_16x16x32_bf16 v[80:83], a[0:3], a[44:47], v[80:83]\n"
      "ds_read_b128 a[88:91], v26 offset:0\n"
      "v_mfma_f32_16x16x32_bf16 v[84:87], a[0:3], a[48:51], v[84:87]\n"
      "v_mfma_f32_16x16x32_bf16 v[88:91], a[0:3], a[52:55], v[88:91]\n"
      "ds_read_b128 a[92:95], v26 offset:2048\n"
      "v_mfma_f32_16x16x32_bf16 v[92:95], a[0:3], a[56:59], v[92:95]\n"
      "v_mfma_f32_16x16x32_bf16 v[124:127], a[4:7], a[56:59], v[124:127]\n"
      "ds_read_b128 a[96:99], v26 offset:4096\n"
      "v_mfma_f32_16x16x32_bf16 v[120:123], a[4:7], a[52:55], v[120:123]\n"
      "v_mfma_f32_16x16x32_bf16 v[116:119], a[4:7], a[48:51], v[116:119]\n"
      "ds_read_b128 a[100:103], v26 offset:6144\n"
      "v_mfma_f32_16x16x32_bf16 v[112:115], a[4:7], a[44:47], v[112:115]\n"
      "v_mfma_f32_16x16x32_bf16 v[108:111], a[4:7], a[40:43], v[108:111]\n"
      "ds_read_b128 a[104:107], v26 offset:8192\n"
      "v_mfma_f32_16x16x32_bf16 v[104:107], a[4:7], a[36:39], v[104:107]\n"
      "v_mfma_f32_16x16x32_bf16 v[100:103], a[4:7], a[32:35], v[100:103]\n"
      "ds_read_b128 a[108:111], v26 offset:10240\n"
      "s_add_u32 m0, s85, s97\n"
      "v_mfma_f32_16x16x32_bf16 v[96:99], a[4:7], a[28:31], v[96:99]\n"
      "buffer_load_dwordx4 v8, s[24:27], s29 offen offset:0 sc1 lds\n"
      "v_mfma_f32_16x16x32_bf16 v[128:131], a[8:11], a[28:31], v[128:131]\n"
      "ds_read_b128 a[112:115], v26 offset:12288\n"
      "v_mfma_f32_16x16x32_bf16 v[132:135], a[8:11], a[32:35], v[132:135]\n"
      "v_mfma_f32_16x16x32_bf16 v[136:139], a[8:11], a[36:39], v[136:139]\n"
      "ds_read_b128 a[116:119], v26 offset:14336\n"
      "v_mfma_f32_16x16x32_bf16 v[140:143], a[8:11], a[40:43], v[140:143]\n"
      "buffer_load_dwordx4 v9, s[24:27], s29 offen offset:1024 sc1 lds\n"
      "v_mfma_f32_16x16x32_bf16 v[144:147], a[8:11], a[44:47], v[144:147]\n"
      "ds_read_b64_tr_b16 a[64:65], v31 offset:14368\n"
      "v_mfma_f32_16x16x32_bf16 v[148:151], a[8:11], a[48:51], v[148:151]\n"
      "v_mfma_f32_16x16x32_bf16 v[152:155], a[8:11], a[52:55], v[152:155]\n"
      "ds_read_b64_tr_b16 a[66:67], v31 offset:16160\n"
      "v_mfma_f32_16x16x32_bf16 v[156:159], a[8:11], a[56:59], v[156:159]\n"
      "buffer_load_dwordx4 v10, s[24:27], s29 offen offset:2048 sc1 lds\n"
      "v_mfma_f32_16x16x32_bf16 v[188:191], a[12:15], a[56:59], v[188:191]\n"
      "ds_read_b64_tr_b16 a[68:69], v30 offset:14400\n"
      "v_mfma_f32_16x16x32_bf16 v[184:187], a[12:15], a[52:55], v[184:187]\n"
      "v_mfma_f32_16x16x32_bf16 v[180:183], a[12:15], a[48:51], v[180:183]\n"
      "ds_read_b64_tr_b16 a[70:71], v30 offset:16192\n"
      "v_mfma_f32_16x16x32_bf16 v[176:179], a[12:15], a[44:47], v[176:179]\n"
      "buffer_load_dwordx4 v11, s[24:27], s29 offen offset:3072 sc1 lds\n"
      "v_mfma_f32_16x16x32_bf16 v[172:175], a[12:15], a[40:43], v[172:175]\n"
      "ds_read_b64_tr_b16 a[72:73], v31 offset:14432\n"
      "v_mfma_f32_16x16x32_bf16 v[168:171], a[12:15], a[36:39], v[168:171]\n"
      "v_mfma_f32_16x16x32_bf16 v[164:167], a[12:15], a[32:35], v[164:167]\n"
      "ds_read_b64_tr_b16 a[74:75], v31 offset:16224\n"
      "s_add_u32 m0, s72, s97\n"
      "v_mfma_f32_16x16x32_bf16 v[160:163], a[12:15], a[28:31], v[160:163]\n"
      "buffer_load_dwordx4 v12, s[24:27], s29 offen offset:0 sc1 lds\n"
      "v_mfma_f32_16x16x32_bf16 v[192:195], a[16:19], a[28:31], v[192:195]\n"
      "ds_read_b64_tr_b16 a[76:77], v30 offset:14464\n"
      "v_mfma_f32_16x16x32_bf16 v[196:199], a[16:19], a[32:35], v[196:199]\n"
      "v_mfma_f32_16x16x32_bf16 v[200:203], a[16:19], a[36:39], v[200:203]\n"
      "ds_read_b64_tr_b16 a[78:79], v30 offset:16256\n"
      "v_mfma_f32_16x16x32_bf16 v[204:207], a[16:19], a[40:43], v[204:207]\n"
      "buffer_load_dwordx4 v13, s[24:27], s29 offen offset:1024 sc1 lds\n"
      "v_mfma_f32_16x16x32_bf16 v[208:211], a[16:19], a[44:47], v[208:211]\n"
      "ds_read_b64_tr_b16 a[80:81], v31 offset:14496\n"
      "v_mfma_f32_16x16x32_bf16 v[212:215], a[16:19], a[48:51], v[212:215]\n"
      "v_mfma_f32_16x16x32_bf16 v[216:219], a[16:19], a[52:55], v[216:219]\n"
      "ds_read_b64_tr_b16 a[82:83], v31 offset:16288\n"
      "v_mfma_f32_16x16x32_bf16 v[220:223], a[16:19], a[56:59], v[220:223]\n"
      "buffer_load_dwordx4 v14, s[24:27], s29 offen offset:2048 sc1 lds\n"
      "v_mfma_f32_16x16x32_bf16 v[252:255], a[20:23], a[56:59], v[252:255]\n"
      "ds_read_b64_tr_b16 a[84:85], v32 offset:14336\n"
      "v_mfma_f32_16x16x32_bf16 v[248:251], a[20:23], a[52:55], v[248:251]\n"
      "v_mfma_f32_16x16x32_bf16 v[244:247], a[20:23], a[48:51], v[244:247]\n"
      "ds_read_b64_tr_b16 a[86:87], v32 offset:16128\n"
      "v_mfma_f32_16x16x32_bf16 v[240:243], a[20:23], a[44:47], v[240:243]\n"
      "buffer_load_dwordx4 v15, s[24:27], s29 offen offset:3072 sc1 lds\n"
      "v_mfma_f32_16x16x32_bf16 v[236:239], a[20:23], a[40:43], v[236:239]\n"
      "v_mfma_f32_16x16x32_bf16 v[232:235], a[20:23], a[36:39], v[232:235]\n"
      "v_mfma_f32_16x16x32_bf16 v[228:231], a[20:23], a[32:35], v[228:231]\n"
      "s_sub_u32 s31, s96, s95\n"
      "v_mfma_f32_16x16x32_bf16 v[224:227], a[20:23], a[28:31], v[224:227]\n"
      "v_add_u32 v25, s31, v25\n"
      "v_mfma_f32_16x16x32_bf16 a[128:131], a[24:27], a[28:31], a[128:131]\n"
      "v_add_u32 v26, s31, v26\n"
      "v_mfma_f32_16x16x32_bf16 a[132:135], a[24:27], a[32:35], a[132:135]\n"
      "s_mov_b32 s31, s95\n"
      "v_mfma_f32_16x16x32_bf16 a[136:139], a[24:27], a[36:39], a[136:139]\n"
      "s_mov_b32 s95, s96\n"
      "v_mfma_f32_16x16x32_bf16 a[140:143], a[24:27], a[40:43], a[140:143]\n"
      "s_mov_b32 s96, s97\n"
      "v_mfma_f32_16x16x32_bf16 a[144:147], a[24:27], a[44:47], a[144:147]\n"
      "s_mov_b32 s97, s31\n"
      "v_mfma_f32_16x16x32_bf16 a[148:151], a[24:27], a[48:51], a[148:151]\n"
      "v_mfma_f32_16x16x32_bf16 a[152:155], a[24:27], a[52:55], a[152:155]\n"
      "v_mfma_f32_16x16x32_bf16 a[156:159], a[24:27], a[56:59], a[156:159]\n"
      "s_waitcnt vmcnt(8) lgkmcnt(0)\n"
      "s_barrier\n"
      "v_mfma_f32_16x16x32_bf16 v[64:67], a[60:63], a[88:91], v[64:67]\n"
      "ds_read_b64_tr_b16 a[0:1], v27 offset:0\n"
      "v_mfma_f32_16x16x32_bf16 v[68:71], a[60:63], a[92:95], v[68:71]\n"
      "v_mfma_f32_16x16x32_bf16 v[72:75], a[60:63], a[96:99], v[72:75]\n"
      "ds_read_b64_tr_b16 a[2:3], v27 offset:1792\n"
      "s_add_u32 m0, s86, 135168\n"
      "v_mfma_f32_16x16x32_bf16 v[76:79], a[60:63], a[100:103], v[76:79]\n"
      "buffer_load_dwordx4 v16, s[20:23], s28 offen offset:0 lds\n"
      "v_mfma_f32_16x16x32_bf16 v[80:83], a[60:63], a[104:107], v[80:83]\n"
      "ds_read_b128 a[28:31], v25 offset:0\n"
      "v_mfma_f32_16x16x32_bf16 v[84:87], a[60:63], a[108:111], v[84:87]\n"
      "v_mfma_f32_16x16x32_bf16 v[88:91], a[60:63], a[112:115], v[88:91]\n"
      "ds_read_b128 a[32:35], v25 offset:2048\n"
      "v_mfma_f32_16x16x32_bf16 v[92:95], a[60:63], a[116:119], v[92:95]\n"
      "buffer_load_dwordx4 v17, s[20:23], s28 offen offset:1024 lds\n"
      "v_mfma_f32_16x16x32_bf16 v[124:127], a[64:67], a[116:119], v[124:127]\n"
      "ds_read_b128 a[36:39], v25 offset:4096\n"
      "v_mfma_f32_16x16x32_bf16 v[120:123], a[64:67], a[112:115], v[120:123]\n"
      "v_mfma_f32_16x16x32_bf16 v[116:119], a[64:67], a[108:111], v[116:119]\n"
      "ds_read_b128 a[40:43], v25 offset:6144\n"
      "v_mfma_f32_16x16x32_bf16 v[112:115], a[64:67], a[104:107], v[112:115]\n"
      "buffer_load_dwordx4 v18, s[20:23], s28 offen offset:2048 lds\n"
      "v_mfma_f32_16x16x32_bf16 v[108:111], a[64:67], a[100:103], v[108:111]\n"
      "ds_read_b128 a[44:47], v25 offset:8192\n"
      "v_mfma_f32_16x16x32_bf16 v[104:107], a[64:67], a[96:99], v[104:107]\n"
      "v_mfma_f32_16x16x32_bf16 v[100:103], a[64:67], a[92:95], v[100:103]\n"
      "ds_read_b128 a[48:51], v25 offset:10240\n"
      "v_mfma_f32_16x16x32_bf16 v[96:99], a[64:67], a[88:91], v[96:99]\n"
      "buffer_load_dwordx4 v19, s[20:23], s28 offen offset:3072 lds\n"
      "v_mfma_f32_16x16x32_bf16 v[128:131], a[68:71], a[88:91], v[128:131]\n"
      "ds_read_b128 a[52:55], v25 offset:12288\n"
      "v_mfma_f32_16x16x32_bf16 v[132:135], a[68:71], a[92:95], v[132:135]\n"
      "v_mfma_f32_16x16x32_bf16 v[136:139], a[68:71], a[96:99], v[136:139]\n"
      "ds_read_b128 a[56:59], v25 offset:14336\n"
      "s_add_u32 m0, s86, 139264\n"
      "v_mfma_f32_16x16x32_bf16 v[140:143], a[68:71], a[100:103], v[140:143]\n"
      "buffer_load_dwordx4 v20, s[20:23], s28 offen offset:0 lds\n"
      "v_mfma_f32_16x16x32_bf16 v[144:147], a[68:71], a[104:107], v[144:147]\n"
      "ds_read_b64_tr_b16 a[4:5], v28 offset:32\n"
      "v_mfma_f32_16x16x32_bf16 v[148:151], a[68:71], a[108:111], v[148:151]\n"
      "v_mfma_f32_16x16x32_bf16 v[152:155], a[68:71], a[112:115], v[152:155]\n"
      "ds_read_b64_tr_b16 a[6:7], v28 offset:1824\n"
      "v_mfma_f32_16x16x32_bf16 v[156:159], a[68:71], a[116:119], v[156:159]\n"
      "buffer_load_dwordx4 v21, s[20:23], s28 offen offset:1024 lds\n"
      "v_mfma_f32_16x16x32_bf16 v[188:191], a[72:75], a[116:119], v[188:191]\n"
      "ds_read_b64_tr_b16 a[8:9], v27 offset:64\n"
      "v_mfma_f32_16x16x32_bf16 v[184:187], a[72:75], a[112:115], v[184:187]\n"
      "v_mfma_f32_16x16x32_bf16 v[180:183], a[72:75], a[108:111], v[180:183]\n"
      "ds_read_b64_tr_b16 a[10:11], v27 offset:1856\n"
      "v_mfma_f32_16x16x32_bf16 v[176:179], a[72:75], a[104:107], v[176:179]\n"
      "buffer_load_dwordx4 v22, s[20:23], s28 offen offset:2048 lds\n"
      "v_mfma_f32_16x16x32_bf16 v[172:175], a[72:75], a[100:103], v[172:175]\n"
      "ds_read_b64_tr_b16 a[12:13], v28 offset:96\n"
      "v_mfma_f32_16x16x32_bf16 v[168:171], a[72:75], a[96:99], v[168:171]\n"
      "s_and_b32 s83, s10, 15\n"
      "s_lshr_b64 s[10:11], s[10:11], 4\n"
      "v_mfma_f32_16x16x32_bf16 v[164:167], a[72:75], a[92:95], v[164:167]\n"
      "ds_read_b64_tr_b16 a[14:15], v28 offset:1888\n"
      "v_mfma_f32_16x16x32_bf16 v[160:163], a[72:75], a[88:91], v[160:163]\n"
      "s_mul_i32 s28, s83, s30\n"
      "s_add_u32 s28, s28, s81\n"
      "v_mfma_f32_16x16x32_bf16 v[192:195], a[76:79], a[88:91], v[192:195]\n"
      "ds_read_b64_tr_b16 a[16:17], v27 offset:128\n"
      "v_mfma_f32_16x16x32_bf16 v[196:199], a[76:79], a[92:95], v[196:199]\n"
      "s_lshl_b32 s29, s83, 7\n"
      "s_add_u32 s29, s29, s82\n"
      "v_mfma_f32_16x16x32_bf16 v[200:203], a[76:79], a[96:99], v[200:203]\n"
      "ds_read_b64_tr_b16 a[18:19], v27 offset:1920\n"
      "v_mfma_f32_16x16x32_bf16 v[204:207], a[76:79], a[100:103], v[204:207]\n"
      "v_mfma_f32_16x16x32_bf16 v[208:211], a[76:79], a[104:107], v[208:211]\n"
      "ds_read_b64_tr_b16 a[20:21], v28 offset:160\n"
      "v_mfma_f32_16x16x32_bf16 v[212:215], a[76:79], a[108:111], v[212:215]\n"
      "v_mfma_f32_16x16x32_bf16 v[216:219], a[76:79], a[112:115], v[216:219]\n"
      "ds_read_b64_tr_b16 a[22:23], v28 offset:1952\n"
      "v_mfma_f32_16x16x32_bf16 v[220:223], a[76:79], a[116:119], v[220:223]\n"
      "v_mfma_f32_16x16x32_bf16 v[252:255], a[80:83], a[116:119], v[252:255]\n"
      "ds_read_b64_tr_b16 a[24:25], v29 offset:0\n"
      "v_mfma_f32_16x16x32_bf16 v[248:251], a[80:83], a[112:115], v[248:251]\n"
      "v_mfma_f32_16x16x32_bf16 v[244:247], a[80:83], a[108:111], v[244:247]\n"
      "ds_read_b64_tr_b16 a[26:27], v29 offset:1792\n"
      "v_mfma_f32_16x16x32_bf16 v[240:243], a[80:83], a[104:107], v[240:243]\n"
      "v_mfma_f32_16x16x32_bf16 v[236:239], a[80:83], a[100:103], v[236:239]\n"
      "v_mfma_f32_16x16x32_bf16 v[232:235], a[80:83], a[96:99], v[232:235]\n"
      "v_mfma_f32_16x16x32_bf16 v[228:231], a[80:83], a[92:95], v[228:231]\n"
      "v_mfma_f32_16x16x32_bf16 v[224:227], a[80:83], a[88:91], v[224:227]\n"
      "v_mfma_f32_16x16x32_bf16 a[128:131], a[84:87], a[88:91], a[128:131]\n"
      "v_mfma_f32_16x16x32_bf16 a[132:135], a[84:87], a[92:95], a[132:135]\n"
      "v_mfma_f32_16x16x32_bf16 a[136:139], a[84:87], a[96:99], a[136:139]\n"
      "v_mfma_f32_16x16x32_bf16 a[140:143], a[84:87], a[100:103], a[140:143]\n"
      "v_mfma_f32_16x16x32_bf16 a[144:147], a[84:87], a[104:107], a[144:147]\n"
      "v_mfma_f32_16x16x32_bf16 a[148:151], a[84:87], a[108:111], a[148:151]\n"
      "v_mfma_f32_16x16x32_bf16 a[152:155], a[84:87], a[112:115], a[152:155]\n"
      "v_mfma_f32_16x16x32_bf16 a[156:159], a[84:87], a[116:119], a[156:159]\n"
      "s_waitcnt lgkmcnt(0)\n"
      "v_mfma_f32_16x16x32_bf16 v[64:67], a[0:3], a[28:31], v[64:67]\n"
      "ds_read_b64_tr_b16 a[60:61], v27 offset:14336\n"
      "v_mfma_f32_16x16x32_bf16 v[68:71], a[0:3], a[32:35], v[68:71]\n"
      "v_mfma_f32_16x16x32_bf16 v[72:75], a[0:3], a[36:39], v[72:75]\n"
      "ds_read_b64_tr_b16 a[62:63], v27 offset:16128\n"
      "v_mfma_f32_16x16x32_bf16 v[76:79], a[0:3], a[40:43], v[76:79]\n"
      "v_mfma_f32_16x16x32_bf16 v[80:83], a[0:3], a[44:47], v[80:83]\n"
      "ds_read_b128 a[88:91], v26 offset:0\n"
      "v_mfma_f32_16x16x32_bf16 v[84:87], a[0:3], a[48:51], v[84:87]\n"
      "v_mfma_f32_16x16x32_bf16 v[88:91], a[0:3], a[52:55], v[88:91]\n"
      "ds_read_b128 a[92:95], v26 offset:2048\n"
      "v_mfma_f32_16x16x32_bf16 v[92:95], a[0:3], a[56:59], v[92:95]\n"
      "v_mfma_f32_16x16x32_bf16 v[124:127], a[4:7], a[56:59], v[124:127]\n"
      "ds_read_b128 a[96:99], v26 offset:4096\n"
      "v_mfma_f32_16x16x32_bf16 v[120:123], a[4:7], a[52:55], v[120:123]\n"
      "v_mfma_f32_16x16x32_bf16 v[116:119], a[4:7], a[48:51], v[116:119]\n"
      "ds_read_b128 a[100:103], v26 offset:6144\n"
      "v_mfma_f32_16x16x32_bf16 v[112:115], a[4:7], a[44:47], v[112:115]\n"
      "v_mfma_f32_16x16x32_bf16 v[108:111], a[4:7], a[40:43], v[108:111]\n"
      "ds_read_b128 a[104:107], v26 offset:8192\n"
      "v_mfma_f32_16x16x32_bf16 v[104:107], a[4:7], a[36:39], v[104:107]\n"
      "v_mfma_f32_16x16x32_bf16 v[100:103], a[4:7], a[32:35], v[100:103]\n"
      "ds_read_b128 a[108:111], v26 offset:10240\n"
      "s_add_u32 m0, s85, s97\n"
      "v_mfma_f32_16x16x32_bf16 v[96:99], a[4:7], a[28:31], v[96:99]\n"
      "buffer_load_dwordx4 v8, s[24:27], s29 offen offset:0 sc1 lds\n"
      "v_mfma_f32_16x16x32_bf16 v[128:131], a[8:11], a[28:31], v[128:131]\n"
      "ds_read_b128 a[112:115], v26 offset:12288\n"
      "v_mfma_f32_16x16x32_bf16 v[132:135], a[8:11], a[32:35], v[132:135]\n"
      "v_mfma_f32_16x16x32_bf16 v[136:139], a[8:11], a[36:39], v[136:139]\n"
      "ds_read_b128 a[116:119], v26 offset:14336\n"
      "v_mfma_f32_16x16x32_bf16 v[140:143], a[8:11], a[40:43], v[140:143]\n"
      "buffer_load_dwordx4 v9, s[24:27], s29 offen offset:1024 sc1 lds\n"
      "v_mfma_f32_16x16x32_bf16 v[144:147], a[8:11], a[44:47], v[144:147]\n"
      "ds_read_b64_tr_b16 a[64:65], v28 offset:14368\n"
      "v_mfma_f32_16x16x32_bf16 v[148:151], a[8:11], a[48:51], v[148:151]\n"
      "v_mfma_f32_16x16x32_bf16 v[152:155], a[8:11], a[52:55], v[152:155]\n"
      "ds_read_b64_tr_b16 a[66:67], v28 offset:16160\n"
      "v_mfma_f32_16x16x32_bf16 v[156:159], a[8:11], a[56:59], v[156:159]\n"
      "buffer_load_dwordx4 v10, s[24:27], s29 offen offset:2048 sc1 lds\n"
      "v_mfma_f32_16x16x32_bf16 v[188:191], a[12:15], a[56:59], v[188:191]\n"
      "ds_read_b64_tr_b16 a[68:69], v27 offset:14400\n"
      "v_mfma_f32_16x16x32_bf16 v[184:187], a[12:15], a[52:55], v[184:187]\n"
      "v_mfma_f32_16x16x32_bf16 v[180:183], a[12:15], a[48:51], v[180:183]\n"
      "ds_read_b64_tr_b16 a[70:71], v27 offset:16192\n"
      "v_mfma_f32_16x16x32_bf16 v[176:179], a[12:15], a[44:47], v[176:179]\n"
      "buffer_load_dwordx4 v11, s[24:27], s29 offen offset:3072 sc1 lds\n"
      "v_mfma_f32_16x16x32_bf16 v[172:175], a[12:15], a[40:43], v[172:175]\n"
      "ds_read_b64_tr_b16 a[72:73], v28 offset:14432\n"
      "v_mfma_f32_16x16x32_bf16 v[168:171], a[12:15], a[36:39], v[168:171]\n"
      "v_mfma_f32_16x16x32_bf16 v[164:167], a[12:15], a[32:35], v[164:167]\n"
      "ds_read_b64_tr_b16 a[74:75], v28 offset:16224\n"
      "s_add_u32 m0, s72, s97\n"
      "v_mfma_f32_16x16x32_bf16 v[160:163], a[12:15], a[28:31], v[160:163]\n"
      "buffer_load_dwordx4 v12, s[24:27], s29 offen offset:0 sc1 lds\n"
      "v_mfma_f32_16x16x32_bf16 v[192:195], a[16:19], a[28:31], v[192:195]\n"
      "ds_read_b64_tr_b16 a[76:77], v27 offset:14464\n"
      "v_mfma_f32_16x16x32_bf16 v[196:199], a[16:19], a[32:35], v[196:199]\n"
      "v_mfma_f32_16x16x32_bf16 v[200:203], a[16:19], a[36:39], v[200:203]\n"
      "ds_read_b64_tr_b16 a[78:79], v27 offset:16256\n"
      "v_mfma_f32_16x16x32_bf16 v[204:207], a[16:19], a[40:43], v[204:207]\n"
      "buffer_load_dwordx4 v13, s[24:27], s29 offen offset:1024 sc1 lds\n"
      "v_mfma_f32_16x16x32_bf16 v[208:211], a[16:19], a[44:47], v[208:211]\n"
      "ds_read_b64_tr_b16 a[80:81], v28 offset:14496\n"
      "v_mfma_f32_16x16x32_bf16 v[212:215], a[16:19], a[48:51], v[212:215]\n"
      "v_mfma_f32_16x16x32_bf16 v[216:219], a[16:19], a[52:55], v[216:219]\n"
      "ds_read_b64_tr_b16 a[82:83], v28 offset:16288\n"
      "v_mfma_f32_16x16x32_bf16 v[220:223], a[16:19], a[56:59], v[220:223]\n"
      "buffer_load_dwordx4 v14, s[24:27], s29 offen offset:2048 sc1 lds\n"
      "v_mfma_f32_16x16x32_bf16 v[252:255], a[20:23], a[56:59], v[252:255]\n"
      "ds_read_b64_tr_b16 a[84:85], v29 offset:14336\n"
      "v_mfma_f32_16x16x32_bf16 v[248:251], a[20:23], a[52:55], v[248:251]\n"
      "v_mfma_f32_16x16x32_bf16 v[244:247], a[20:23], a[48:51], v[244:247]\n"
      "ds_read_b64_tr_b16 a[86:87], v29 offset:16128\n"
      "v_mfma_f32_16x16x32_bf16 v[240:243], a[20:23], a[44:47], v[240:243]\n"
      "buffer_load_dwordx4 v15, s[24:27], s29 offen offset:3072 sc1 lds\n"
      "v_mfma_f32_16x16x32_bf16 v[236:239], a[20:23], a[40:43], v[236:239]\n"
      "v_mfma_f32_16x16x32_bf16 v[232:235], a[20:23], a[36:39], v[232:235]\n"
      "v_mfma_f32_16x16x32_bf16 v[228:231], a[20:23], a[32:35], v[228:231]\n"
      "s_sub_u32 s31, s96, s95\n"
      "v_mfma_f32_16x16x32_bf16 v[224:227], a[20:23], a[28:31], v[224:227]\n"
      "v_add_u32 v25, s31, v25\n"
      "v_mfma_f32_16x16x32_bf16 a[128:131], a[24:27], a[28:31], a[128:131]\n"
      "v_add_u32 v26, s31, v26\n"
      "v_mfma_f32_16x16x32_bf16 a[132:135], a[24:27], a[32:35], a[132:135]\n"
      "s_mov_b32 s31, s95\n"
      "v_mfma_f32_16x16x32_bf16 a[136:139], a[24:27], a[36:39], a[136:139]\n"
      "s_mov_b32 s95, s96\n"
      "v_mfma_f32_16x16x32_bf16 a[140:143], a[24:27], a[40:43], a[140:143]\n"
      "s_mov_b32 s96, s97\n"
      "v_mfma_f32_16x16x32_bf16 a[144:147], a[24:27], a[44:47], a[144:147]\n"
      "s_mov_b32 s97, s31\n"
      "v_mfma_f32_16x16x32_bf16 a[148:151], a[24:27], a[48:51], a[148:151]\n"
      "v_mfma_f32_16x16x32_bf16 a[152:155], a[24:27], a[52:55], a[152:155]\n"
      "v_mfma_f32_16x16x32_bf16 a[156:159], a[24:27], a[56:59], a[156:159]\n"
      "s_sub_u32 s93, s93, 1\n"
      "s_cmp_lg_u32 s93, 0\n"
      "s_cbranch_scc1 Lk_g1h\n"
      "s_waitcnt vmcnt(8) lgkmcnt(0)\n"
      "s_barrier\n"
      "v_mfma_f32_16x16x32_bf16 v[64:67], a[60:63], a[88:91], v[64:67]\n"
      "ds_read_b64_tr_b16 a[0:1], v30 offset:0\n"
      "v_mfma_f32_16x16x32_bf16 v[68:71], a[60:63], a[92:95], v[68:71]\n"
      "v_mfma_f32_16x16x32_bf16 v[72:75], a[60:63], a[96:99], v[72:75]\n"
      "ds_read_b64_tr_b16 a[2:3], v30 offset:1792\n"
      "s_add_u32 m0, s86, 73728\n"
      "v_mfma_f32_16x16x32_bf16 v[76:79], a[60:63], a[100:103], v[76:79]\n"
      "buffer_load_dwordx4 v16, s[20:23], s28 offen offset:0 lds\n"
      "v_mfma_f32_16x16x32_bf16 v[80:83], a[60:63], a[104:107], v[80:83]\n"
      "ds_read_b128 a[28:31], v25 offset:0\n"
      "v_mfma_f32_16x16x32_bf16 v[84:87], a[60:63], a[108:111], v[84:87]\n"
      "v_mfma_f32_16x16x32_bf16 v[88:91], a[60:63], a[112:115], v[88:91]\n"
      "ds_read_b128 a[32:35], v25 offset:2048\n"
      "v_mfma_f32_16x16x32_bf16 v[92:95], a[60:63], a[116:119], v[92:95]\n"
      "buffer_load_dwordx4 v17, s[20:23], s28 offen offset:1024 lds\n"
      "v_mfma_f32_16x16x32_bf16 v[124:127], a[64:67], a[116:119], v[124:127]\n"
      "ds_read_b128 a[36:39], v25 offset:4096\n"
      "v_mfma_f32_16x16x32_bf16 v[120:123], a[64:67], a[112:115], v[120:123]\n"
      "v_mfma_f32_16x16x32_bf16 v[116:119], a[64:67], a[108:111], v[116:119]\n"
      "ds_read_b128 a[40:43], v25 offset:6144\n"
      "v_mfma_f32_16x16x32_bf16 v[112:115], a[64:67], a[104:107], v[112:115]\n"
      "buffer_load_dwordx4 v18, s[20:23], s28 offen offset:2048 lds\n"
      "v_mfma_f32_16x16x32_bf16 v[108:111], a[64:67], a[100:103], v[108:111]\n"
      "ds_read_b128 a[44:47], v25 offset:8192\n"
      "v_mfma_f32_16x16x32_bf16 v[104:107], a[64:67], a[96:99], v[104:107]\n"
      "v_mfma_f32_16x16x32_bf16 v[100:103], a[64:67], a[92:95], v[100:103]\n"
      "ds_read_b128 a[48:51], v25 offset:10240\n"
      "v_mfma_f32_16x16x32_bf16 v[96:99], a[64:67], a[88:91], v[96:99]\n"
      "buffer_load_dwordx4 v19, s[20:23], s28 offen offset:3072 lds\n"
      "v_mfma_f32_16x16x32_bf16 v[128:131], a[68:71], a[88:91], v[128:131]\n"
      "ds_read_b128 a[52:55], v25 offset:12288\n"
      "v_mfma_f32_16x16x32_bf16 v[132:135], a[68:71], a[92:95], v[132:135]\n"
      "v_mfma_f32_16x16x32_bf16 v[136:139], a[68:71], a[96:99], v[136:139]\n"
      "ds_read_b128 a[56:59], v25 offset:14336\n"
      "s_add_u32 m0, s86, 77824\n"
      "v_mfma_f32_16x16x32_bf16 v[140:143], a[68:71], a[100:103], v[140:143]\n"
      "buffer_load_dwordx4 v20, s[20:23], s28 offen offset:0 lds\n"
      "v_mfma_f32_16x16x32_bf16 v[144:147], a[68:71], a[104:107], v[144:147]\n"
      "ds_read_b64_tr_b16 a[4:5], v31 offset:32\n"
      "v_mfma_f32_16x16x32_bf16 v[148:151], a[68:71], a[108:111], v[148:151]\n"
      "v_mfma_f32_16x16x32_bf16 v[152:155], a[68:71], a[112:115], v[152:155]\n"
      "ds_read_b64_tr_b16 a[6:7], v31 offset:1824\n"
      "v_mfma_f32_16x16x32_bf16 v[156:159], a[68:71], a[116:119], v[156:159]\n"
      "buffer_load_dwordx4 v21, s[20:23], s28 offen offset:1024 lds\n"
      "v_mfma_f32_16x16x32_bf16 v[188:191], a[72:75], a[116:119], v[188:191]\n"
      "ds_read_b64_tr_b16 a[8:9], v30 offset:64\n"
      "v_mfma_f32_16x16x32_bf16 v[184:187], a[72:75], a[112:115], v[184:187]\n"
      "v_mfma_f32_16x16x32_bf16 v[180:183], a[72:75], a[108:111], v[180:183]\n"
      "ds_read_b64_tr_b16 a[10:11], v30 offset:1856\n"
      "v_mfma_f32_16x16x32_bf16 v[176:179], a[72:75], a[104:107], v[176:179]\n"
      "buffer_load_dwordx4 v22, s[20:23], s28 offen offset:2048 lds\n"
      "v_mfma_f32_16x16x32_bf16 v[172:175], a[72:75], a[100:103], v[172:175]\n"
      "ds_read_b64_tr_b16 a[12:13], v31 offset:96\n"
      "v_mfma_f32_16x16x32_bf16 v[168:171], a[72:75], a[96:99], v[168:171]\n"
      "s_and_b32 s83, s10, 15\n"
      "s_lshr_b64 s[10:11], s[10:11], 4\n"
      "v_mfma_f32_16x16x32_bf16 v[164:167], a[72:75], a[92:95], v[164:167]\n"
      "ds_read_b64_tr_b16 a[14:15], v31 offset:1888\n"
      "v_mfma_f32_16x16x32_bf16 v[160:163], a[72:75], a[88:91], v[160:163]\n"
      "s_mul_i32 s28, s83, s30\n"
      "s_add_u32 s28, s28, s81\n"
      "v_mfma_f32_16x16x32_bf16 v[192:195], a[76:79], a[88:91], v[192:195]\n"
      "ds_read_b64_tr_b16 a[16:17], v30 offset:128\n"
      "v_mfma_f32_16x16x32_bf16 v[196:199], a[76:79], a[92:95], v[196:199]\n"
      "s_lshl_b32 s29, s83, 7\n"
      "s_add_u32 s29, s29, s82\n"
      "v_mfma_f32_16x16x32_bf16 v[200:203], a[76:79], a[96:99], v[200:203]\n"
      "ds_read_b64_tr_b16 a[18:19], v30 offset:1920\n"
      "v_mfma_f32_16x16x32_bf16 v[204:207], a[76:79], a[100:103], v[204:207]\n"
      "v_mfma_f32_16x16x32_bf16 v[208:211], a[76:79], a[104:107], v[208:211]\n"
      "ds_read_b64_tr_b16 a[20:21], v31 offset:160\n"
      "v_mfma_f32_16x16x32_bf16 v[212:215], a[76:79], a[108:111], v[212:215]\n"
      "v_mfma_f32_16x16x32_bf16 v[216:219], a[76:79], a[112:115], v[216:219]\n"
      "ds_read_b64_tr_b16 a[22:23], v31 offset:1952\n"
      "v_mfma_f32_16x16x32_bf16 v[220:223], a[76:79], a[116:119], v[220:223]\n"
      "v_mfma_f32_16x16x32_bf16 v[252:255], a[80:83], a[116:119], v[252:255]\n"
      "ds_read_b64_tr_b16 a[24:25], v32 offset:0\n"
      "v_mfma_f32_16x16x32_bf16 v[248:251], a[80:83], a[112:115], v[248:251]\n"
      "v_mfma_f32_16x16x32_bf16 v[244:247], a[80:83], a[108:111], v[244:247]\n"
      "ds_read_b64_tr_b16 a[26:27], v32 offset:1792\n"
      "v_mfma_f32_16x16x32_bf16 v[240:243], a[80:83], a[104:107], v[240:243]\n"
      "v_mfma_f32_16x16x32_bf16 v[236:239], a[80:83], a[100:103], v[236:239]\n"
      "v_mfma_f32_16x16x32_bf16 v[232:235], a[80:83], a[96:99], v[232:235]\n"
      "v_mfma_f32_16x16x32_bf16 v[228:231], a[80:83], a[92:95], v[228:231]\n"
      "v_mfma_f32_16x16x32_bf16 v[224:227], a[80:83], a[88:91], v[224:227]\n"
      "v_mfma_f32_16x16x32_bf16 a[128:131], a[84:87], a[88:91], a[128:131]\n"
      "v_mfma_f32_16x16x32_bf16 a[132:135], a[84:87], a[92:95], a[132:135]\n"
      "v_mfma_f32_16x16x32_bf16 a[136:139], a[84:87], a[96:99], a[136:139]\n"
      "v_mfma_f32_16x16x32_bf16 a[140:143], a[84:87], a[100:103], a[140:143]\n"
      "v_mfma_f32_16x16x32_bf16 a[144:147], a[84:87], a[104:107], a[144:147]\n"
      "v_mfma_f32_16x16x32_bf16 a[148:151], a[84:87], a[108:111], a[148:151]\n"
      "v_mfma_f32_16x16x32_bf16 a[152:155], a[84:87], a[112:115], a[152:155]\n"
      "v_mfma_f32_16x16x32_bf16 a[156:159], a[84:87], a[116:119], a[156:159]\n"
      "s_waitcnt lgkmcnt(0)\n"
      "v_mfma_f32_16x16x32_bf16 v[64:67], a[0:3], a[28:31], v[64:67]\n"
      "ds_read_b64_tr_b16 a[60:61], v30 offset:14336\n"
      "v_mfma_f32_16x16x32_bf16 v[68:71], a[0:3], a[32:35], v[68:71]\n"
      "v_mfma_f32_16x16x32_bf16 v[72:75], a[0:3], a[36:39], v[72:75]\n"
      "ds_read_b64_tr_b16 a[62:63], v30 offset:16128\n"
      "v_mfma_f32_16x16x32_bf16 v[76:79], a[0:3], a[40:43], v[76:79]\n"
      "v_mfma_f32_16x16x32_bf16 v[80:83], a[0:3], a[44:47], v[80:83]\n"
      "ds_read_b128 a[88:91], v26 offset:0\n"
      "v_mfma_f32_16x16x32_bf16 v[84:87], a[0:3], a[48:51], v[84:87]\n"
      "v_mfma_f32_16x16x32_bf16 v[88:91], a[0:3], a[52:55], v[88:91]\n"
      "ds_read_b128 a[92:95], v26 offset:2048\n"
      "v_mfma_f32_16x16x32_bf16 v[92:95], a[0:3], a[56:59], v[92:95]\n"
      "v_mfma_f32_16x16x32_bf16 v[124:127], a[4:7], a[56:59], v[124:127]\n"
      "ds_read_b128 a[96:99], v26 offset:4096\n"
      "v_mfma_f32_16x16x32_bf16 v[120:123], a[4:7], a[52:55], v[120:123]\n"
      "v_mfma_f32_16x16x32_bf16 v[116:119], a[4:7], a[48:51], v[116:119]\n"
      "ds_read_b128 a[100:103], v26 offset:6144\n"
      "v_mfma_f32_16x16x32_bf16 v[112:115], a[4:7], a[44:47], v[112:115]\n"
      "v_mfma_f32_16x16x32_bf16 v[108:111], a[4:7], a[40:43], v[108:111]\n"
      "ds_read_b128 a[104:107], v26 offset:8192\n"
      "v_mfma_f32_16x16x32_bf16 v[104:107], a[4:7], a[36:39], v[104:107]\n"
      "v_mfma_f32_16x16x32_bf16 v[100:103], a[4:7], a[32:35], v[100:103]\n"
      "ds_read_b128 a[108:111], v26 offset:10240\n"
      "s_add_u32 m0, s85, s97\n"
      "v_mfma_f32_16x16x32_bf16 v[96:99], a[4:7], a[28:31], v[96:99]\n"
      "buffer_load_dwordx4 v8, s[24:27], s29 offen offset:0 sc1 lds\n"
      "v_mfma_f32_16x16x32_bf16 v[128:131], a[8:11], a[28:31], v[128:131]\n"
      "ds_read_b128 a[112:115], v26 offset:12288\n"
      "v_mfma_f32_16x16x32_bf16 v[132:135], a[8:11], a[32:35], v[132:135]\n"
      "v_mfma_f32_16x16x32_bf16 v[136:139], a[8:11], a[36:39], v[136:139]\n"
      "ds_read_b128 a[116:119], v26 offset:14336\n"
      "v_mfma_f32_16x16x32_bf16 v[140:143], a[8:11], a[40:43], v[140:143]\n"
      "buffer_load_dwordx4 v9, s[24:27], s29 offen offset:1024 sc1 lds\n"
      "v_mfma_f32_16x16x32_bf16 v[144:147], a[8:11], a[44:47], v[144:147]\n"
      "ds_read_b64_tr_b16 a[64:65], v31 offset:14368\n"
      "v_mfma_f32_16x16x32_bf16 v[148:151], a[8:11], a[48:51], v[148:151]\n"
      "v_mfma_f32_16x16x32_bf16 v[152:155], a[8:11], a[52:55], v[152:155]\n"
      "ds_read_b64_tr_b16 a[66:67], v31 offset:16160\n"
      "v_mfma_f32_16x16x32_bf16 v[156:159], a[8:11], a[56:59], v[156:159]\n"
      "buffer_load_dwordx4 v10, s[24:27], s29 offen offset:2048 sc1 lds\n"
      "v_mfma_f32_16x16x32_bf16 v[188:191], a[12:15], a[56:59], v[188:191]\n"
      "ds_read_b64_tr_b16 a[68:69], v30 offset:14400\n"
      "v_mfma_f32_16x16x32_bf16 v[184:187], a[12:15], a[52:55], v[184:187]\n"
      "v_mfma_f32_16x16x32_bf16 v[180:183], a[12:15], a[48:51], v[180:183]\n"
      "ds_read_b64_tr_b16 a[70:71], v30 offset:16192\n"
      "v_mfma_f32_16x16x32_bf16 v[176:179], a[12:15], a[44:47], v[176:179]\n"
      "buffer_load_dwordx4 v11, s[24:27], s29 offen offset:3072 sc1 lds\n"
      "v_mfma_f32_16x16x32_bf16 v[172:175], a[12:15], a[40:43], v[172:175]\n"
      "ds_read_b64_tr_b16 a[72:73], v31 offset:14432\n"
      "v_mfma_f32_16x16x32_bf16 v[168:171], a[12:15], a[36:39], v[168:171]\n"
      "v_mfma_f32_16x16x32_bf16 v[164:167], a[12:15], a[32:35], v[164:167]\n"
      "ds_read_b64_tr_b16 a[74:75], v31 offset:16224\n"
      "s_add_u32 m0, s72, s97\n"
      "v_mfma_f32_16x16x32_bf16 v[160:163], a[12:15], a[28:31], v[160:163]\n"
      "buffer_load_dwordx4 v12, s[24:27], s29 offen offset:0 sc1 lds\n"
      "v_mfma_f32_16x16x32_bf16 v[192:195], a[16:19], a[28:31], v[192:195]\n"
      "ds_read_b64_tr_b16 a[76:77], v30 offset:14464\n"
      "v_mfma_f32_16x16x32_bf16 v[196:199], a[16:19], a[32:35], v[196:199]\n"
      "v_mfma_f32_16x16x32_bf16 v[200:203], a[16:19], a[36:39], v[200:203]\n"
      "ds_read_b64_tr_b16 a[78:79], v30 offset:16256\n"
      "v_mfma_f32_16x16x32_bf16 v[204:207], a[16:19], a[40:43], v[204:207]\n"
      "buffer_load_dwordx4 v13, s[24:27], s29 offen offset:1024 sc1 lds\n"
      "v_mfma_f32_16x16x32_bf16 v[208:211], a[16:19], a[44:47], v[208:211]\n"
      "ds_read_b64_tr_b16 a[80:81], v31 offset:14496\n"
      "v_mfma_f32_16x16x32_bf16 v[212:215], a[16:19], a[48:51], v[212:215]\n"
      "v_mfma_f32_16x16x32_bf16 v[216:219], a[16:19], a[52:55], v[216:219]\n"
      "ds_read_b64_tr_b16 a[82:83], v31 offset:16288\n"
      "v_mfma_f32_16x16x32_bf16 v[220:223], a[16:19], a[56:59], v[220:223]\n"
      "buffer_load_dwordx4 v14, s[24:27], s29 offen offset:2048 sc1 lds\n"
      "v_mfma_f32_16x16x32_bf16 v[252:255], a[20:23], a[56:59], v[252:255]\n"
      "ds_read_b64_tr_b16 a[84:85], v32 offset:14336\n"
      "v_mfma_f32_16x16x32_bf16 v[248:251], a[20:23], a[52:55], v[248:251]\n"
      "v_mfma_f32_16x16x32_bf16 v[244:247], a[20:23], a[48:51], v[244:247]\n"
      "ds_read_b64_tr_b16 a[86:87], v32 offset:16128\n"
      "v_mfma_f32_16x16x32_bf16 v[240:243], a[20:23], a[44:47], v[240:243]\n"
      "buffer_load_dwordx4 v15, s[24:27], s29 offen offset:3072 sc1 lds\n"
      "v_mfma_f32_16x16x32_bf16 v[236:239], a[20:23], a[40:43], v[236:239]\n"
      "v_mfma_f32_16x16x32_bf16 v[232:235], a[20:23], a[36:39], v[232:235]\n"
      "v_mfma_f32_16x16x32_bf16 v[228:231], a[20:23], a[32:35], v[228:231]\n"
      "s_sub_u32 s31, s96, s95\n"
      "v_mfma_f32_16x16x32_bf16 v[224:227], a[20:23], a[28:31], v[224:227]\n"
      "v_add_u32 v25, s31, v25\n"
      "v_mfma_f32_16x16x32_bf16 a[128:131], a[24:27], a[28:31], a[128:131]\n"
      "v_add_u32 v26, s31, v26\n"
      "v_mfma_f32_16x16x32_bf16 a[132:135], a[24:27], a[32:35], a[132:135]\n"
      "s_mov_b32 s31, s95\n"
      "v_mfma_f32_16x16x32_bf16 a[136:139], a[24:27], a[36:39], a[136:139]\n"
      "s_mov_b32 s95, s96\n"
      "v_mfma_f32_16x16x32_bf16 a[140:143], a[24:27], a[40:43], a[140:143]\n"
      "s_mov_b32 s96, s97\n"
      "v_mfma_f32_16x16x32_bf16 a[144:147], a[24:27], a[44:47], a[144:147]\n"
      "s_mov_b32 s97, s31\n"
      "v_mfma_f32_16x16x32_bf16 a[148:151], a[24:27], a[48:51], a[148:151]\n"
      "v_mfma_f32_16x16x32_bf16 a[152:155], a[24:27], a[52:55], a[152:155]\n"
      "v_mfma_f32_16x16x32_bf16 a[156:159], a[24:27], a[56:59], a[156:159]\n"
      "s_waitcnt vmcnt(8) lgkmcnt(0)\n"
      "s_barrier\n"
      "v_mfma_f32_16x16x32_bf16 v[64:67], a[60:63], a[88:91], v[64:67]\n"
      "ds_read_b64_tr_b16 a[0:1], v27 offset:0\n"
      "buffer_load_dwordx2 v[56:57], v55, s[64:67], s80 offen sc1\n"
      "v_mfma_f32_16x16x32_bf16 v[68:71], a[60:63], a[92:95], v[68:71]\n"
      "v_mfma_f32_16x16x32_bf16 v[72:75], a[60:63], a[96:99], v[72:75]\n"
      "ds_read_b64_tr_b16 a[2:3], v27 offset:1792\n"
      "s_add_u32 m0, s86, 135168\n"
      "v_mfma_f32_16x16x32_bf16 v[76:79], a[60:63], a[100:103], v[76:79]\n"
      "buffer_load_dwordx4 v16, s[20:23], s28 offen offset:0 lds\n"
      "v_mfma_f32_16x16x32_bf16 v[80:83], a[60:63], a[104:107], v[80:83]\n"
      "ds_read_b128 a[28:31], v25 offset:0\n"
      "v_mfma_f32_16x16x32_bf16 v[84:87], a[60:63], a[108:111], v[84:87]\n"
      "v_mfma_f32_16x16x32_bf16 v[88:91], a[60:63], a[112:115], v[88:91]\n"
      "ds_read_b128 a[32:35], v25 offset:2048\n"
      "v_mfma_f32_16x16x32_bf16 v[92:95], a[60:63], a[116:119], v[92:95]\n"
      "buffer_load_dwordx4 v17, s[20:23], s28 offen offset:1024 lds\n"
      "v_mfma_f32_16x16x32_bf16 v[124:127], a[64:67], a[116:119], v[124:127]\n"
      "ds_read_b128 a[36:39], v25 offset:4096\n"
      "v_mfma_f32_16x16x32_bf16 v[120:123], a[64:67], a[112:115], v[120:123]\n"
      "v_mfma_f32_16x16x32_bf16 v[116:119], a[64:67], a[108:111], v[116:119]\n"
      "ds_read_b128 a[40:43], v25 offset:6144\n"
      "v_mfma_f32_16x16x32_bf16 v[112:115], a[64:67], a[104:107], v[112:115]\n"
      "buffer_load_dwordx4 v18, s[20:23], s28 offen offset:2048 lds\n"
      "v_mfma_f32_16x16x32_bf16 v[108:111], a[64:67], a[100:103], v[108:111]\n"
      "ds_read_b128 a[44:47], v25 offset:8192\n"
      "v_mfma_f32_16x16x32_bf16 v[104:107], a[64:67], a[96:99], v[104:107]\n"
      "v_mfma_f32_16x16x32_bf16 v[100:103], a[64:67], a[92:95], v[100:103]\n"
      "ds_read_b128 a[48:51], v25 offset:10240\n"
      "v_mfma_f32_16x16x32_bf16 v[96:99], a[64:67], a[88:91], v[96:99]\n"
      "buffer_load_dwordx4 v19, s[20:23], s28 offen offset:3072 lds\n"
      "v_mfma_f32_16x16x32_bf16 v[128:131], a[68:71], a[88:91], v[128:131]\n"
      "ds_read_b128 a[52:55], v25 offset:12288\n"
      "v_mfma_f32_16x16x32_bf16 v[132:135], a[68:71], a[92:95], v[132:135]\n"
      "v_mfma_f32_16x16x32_bf16 v[136:139], a[68:71], a[96:99], v[136:139]\n"
      "ds_read_b128 a[56:59], v25 offset:14336\n"
      "s_add_u32 m0, s86, 139264\n"
      "v_mfma_f32_16x16x32_bf16 v[140:143], a[68:71], a[100:103], v[140:143]\n"
      "buffer_load_dwordx4 v20, s[20:23], s28 offen offset:0 lds\n"
      "v_mfma_f32_16x16x32_bf16 v[144:147], a[68:71], a[104:107], v[144:147]\n"
      "ds_read_b64_tr_b16 a[4:5], v28 offset:32\n"
      "v_mfma_f32_16x16x32_bf16 v[148:151], a[68:71], a[108:111], v[148:151]\n"
      "v_mfma_f32_16x16x32_bf16 v[152:155], a[68:71], a[112:115], v[152:155]\n"
      "ds_read_b64_tr_b16 a[6:7], v28 offset:1824\n"
      "v_mfma_f32_16x16x32_bf16 v[156:159], a[68:71], a[116:119], v[156:159]\n"
      "buffer_load_dwordx4 v21, s[20:23], s28 offen offset:1024 lds\n"
      "v_mfma_f32_16x16x32_bf16 v[188:191], a[72:75], a[116:119], v[188:191]\n"
      "ds_read_b64_tr_b16 a[8:9], v27 offset:64\n"
      "v_mfma_f32_16x16x32_bf16 v[184:187], a[72:75], a[112:115], v[184:187]\n"
      "v_mfma_f32_16x16x32_bf16 v[180:183], a[72:75], a[108:111], v[180:183]\n"
      "ds_read_b64_tr_b16 a[10:11], v27 offset:1856\n"
      "v_mfma_f32_16x16x32_bf16 v[176:179], a[72:75], a[104:107], v[176:179]\n"
      "buffer_load_dwordx4 v22, s[20:23], s28 offen offset:2048 lds\n"
      "v_mfma_f32_16x16x32_bf16 v[172:175], a[72:75], a[100:103], v[172:175]\n"
      "ds_read_b64_tr_b16 a[12:13], v28 offset:96\n"
      "v_mfma_f32_16x16x32_bf16 v[168:171], a[72:75], a[96:99], v[168:171]\n"
      "v_mfma_f32_16x16x32_bf16 v[164:167], a[72:75], a[92:95], v[164:167]\n"
      "ds_read_b64_tr_b16 a[14:15], v28 offset:1888\n"
      "v_mfma_f32_16x16x32_bf16 v[160:163], a[72:75], a[88:91], v[160:163]\n"
      "v_mfma_f32_16x16x32_bf16 v[192:195], a[76:79], a[88:91], v[192:195]\n"
      "ds_read_b64_tr_b16 a[16:17], v27 offset:128\n"
      "v_mfma_f32_16x16x32_bf16 v[196:199], a[76:79], a[92:95], v[196:199]\n"
      "v_mfma_f32_16x16x32_bf16 v[200:203], a[76:79], a[96:99], v[200:203]\n"
      "ds_read_b64_tr_b16 a[18:19], v27 offset:1920\n"
      "s_movk_i32 s30, 16384\n"
      "v_mfma_f32_16x16x32_bf16 v[204:207], a[76:79], a[100:103], v[204:207]\n"
      "s_mov_b32 s26, 0x800000\n"
      "s_mov_b32 s24, s40\n"
      "v_mfma_f32_16x16x32_bf16 v[208:211], a[76:79], a[104:107], v[208:211]\n"
      "ds_read_b64_tr_b16 a[20:21], v28 offset:160\n"
      "s_and_b32 s25, s41, 0xffff\n"
      "v_mfma_f32_16x16x32_bf16 v[212:215], a[76:79], a[108:111], v[212:215]\n"
      "s_mov_b32 s81, s78\n"
      "s_mov_b32 s82, s79\n"
      "v_mfma_f32_16x16x32_bf16 v[216:219], a[76:79], a[112:115], v[216:219]\n"
      "ds_read_b64_tr_b16 a[22:23], v28 offset:1952\n"
      "s_lshr_b64 s[10:11], s[16:17], 0\n"
      "v_mfma_f32_16x16x32_bf16 v[220:223], a[76:79], a[116:119], v[220:223]\n"
      "s_and_b32 s83, s10, 15\n"
      "s_lshr_b64 s[10:11], s[10:11], 4\n"
      "v_mfma_f32_16x16x32_bf16 v[252:255], a[80:83], a[116:119], v[252:255]\n"
      "ds_read_b64_tr_b16 a[24:25], v29 offset:0\n"
      "s_mul_i32 s28, s83, s30\n"
      "v_mfma_f32_16x16x32_bf16 v[248:251], a[80:83], a[112:115], v[248:251]\n"
      "s_add_u32 s28, s28, s81\n"
      "s_lshl_b32 s29, s83, 7\n"
      "v_mfma_f32_16x16x32_bf16 v[244:247], a[80:83], a[108:111], v[244:247]\n"
      "ds_read_b64_tr_b16 a[26:27], v29 offset:1792\n"
      "s_add_u32 s29, s29, s82\n"
      "v_mfma_f32_16x16x32_bf16 v[240:243], a[80:83], a[104:107], v[240:243]\n"
      "v_mfma_f32_16x16x32_bf16 v[236:239], a[80:83], a[100:103], v[236:239]\n"
      "v_mfma_f32_16x16x32_bf16 v[232:235], a[80:83], a[96:99], v[232:235]\n"
      "v_mfma_f32_16x16x32_bf16 v[228:231], a[80:83], a[92:95], v[228:231]\n"
      "v_mfma_f32_16x16x32_bf16 v[224:227], a[80:83], a[88:91], v[224:227]\n"
      "v_mfma_f32_16x16x32_bf16 a[128:131], a[84:87], a[88:91], a[128:131]\n"
      "v_mfma_f32_16x16x32_bf16 a[132:135], a[84:87], a[92:95], a[132:135]\n"
      "v_mfma_f32_16x16x32_bf16 a[136:139], a[84:87], a[96:99], a[136:139]\n"
      "v_mfma_f32_16x16x32_bf16 a[140:143], a[84:87], a[100:103], a[140:143]\n"
      "v_mfma_f32_16x16x32_bf16 a[144:147], a[84:87], a[104:107], a[144:147]\n"
      "v_mfma_f32_16x16x32_bf16 a[148:151], a[84:87], a[108:111], a[148:151]\n"
      "v_mfma_f32_16x16x32_bf16 a[152:155], a[84:87], a[112:115], a[152:155]\n"
      "v_mfma_f32_16x16x32_bf16 a[156:159], a[84:87], a[116:119], a[156:159]\n"
      "s_waitcnt lgkmcnt(0)\n"
      "v_mfma_f32_16x16x32_bf16 v[64:67], a[0:3], a[28:31], v[64:67]\n"
      "ds_read_b64_tr_b16 a[60:61], v27 offset:14336\n"
      "v_mfma_f32_16x16x32_bf16 v[68:71], a[0:3], a[32:35], v[68:71]\n"
      "v_mfma_f32_16x16x32_bf16 v[72:75], a[0:3], a[36:39], v[72:75]\n"
      "ds_read_b64_tr_b16 a[62:63], v27 offset:16128\n"
      "v_mfma_f32_16x16x32_bf16 v[76:79], a[0:3], a[40:43], v[76:79]\n"
      "v_mfma_f32_16x16x32_bf16 v[80:83], a[0:3], a[44:47], v[80:83]\n"
      "ds_read_b128 a[88:91], v26 offset:0\n"
      "v_mfma_f32_16x16x32_bf16 v[84:87], a[0:3], a[48:51], v[84:87]\n"
      "v_mfma_f32_16x16x32_bf16 v[88:91], a[0:3], a[52:55], v[88:91]\n"
      "ds_read_b128 a[92:95], v26 offset:2048\n"
      "v_mfma_f32_16x16x32_bf16 v[92:95], a[0:3], a[56:59], v[92:95]\n"
      "v_mfma_f32_16x16x32_bf16 v[124:127], a[4:7], a[56:59], v[124:127]\n"
      "ds_read_b128 a[96:99], v26 offset:4096\n"
      "v_mfma_f32_16x16x32_bf16 v[120:123], a[4:7], a[52:55], v[120:123]\n"
      "v_mfma_f32_16x16x32_bf16 v[116:119], a[4:7], a[48:51], v[116:119]\n"
      "ds_read_b128 a[100:103], v26 offset:6144\n"
      "v_mfma_f32_16x16x32_bf16 v[112:115], a[4:7], a[44:47], v[112:115]\n"
      "v_mfma_f32_16x16x32_bf16 v[108:111], a[4:7], a[40:43], v[108:111]\n"
      "ds_read_b128 a[104:107], v26 offset:8192\n"
      "v_mfma_f32_16x16x32_bf16 v[104:107], a[4:7], a[36:39], v[104:107]\n"
      "v_mfma_f32_16x16x32_bf16 v[100:103], a[4:7], a[32:35], v[100:103]\n"
      "ds_read_b128 a[108:111], v26 offset:10240\n"
      "s_add_u32 m0, s85, 0\n"
      "v_mfma_f32_16x16x32_bf16 v[96:99], a[4:7], a[28:31], v[96:99]\n"
      "buffer_load_dwordx4 v37, s[24:27], s29 offen offset:0 lds\n"
      "v_mfma_f32_16x16x32_bf16 v[128:131], a[8:11], a[28:31], v[128:131]\n"
      "ds_read_b128 a[112:115], v26 offset:12288\n"
      "v_mfma_f32_16x16x32_bf16 v[132:135], a[8:11], a[32:35], v[132:135]\n"
      "v_mfma_f32_16x16x32_bf16 v[136:139], a[8:11], a[36:39], v[136:139]\n"
      "ds_read_b128 a[116:119], v26 offset:14336\n"
      "v_mfma_f32_16x16x32_bf16 v[140:143], a[8:11], a[40:43], v[140:143]\n"
      "buffer_load_dwordx4 v38, s[24:27], s29 offen offset:1024 lds\n"
      "v_mfma_f32_16x16x32_bf16 v[144:147], a[8:11], a[44:47], v[144:147]\n"
      "ds_read_b64_tr_b16 a[64:65], v28 offset:14368\n"
      "v_mfma_f32_16x16x32_bf16 v[148:151], a[8:11], a[48:51], v[148:151]\n"
      "v_mfma_f32_16x16x32_bf16 v[152:155], a[8:11], a[52:55], v[152:155]\n"
      "ds_read_b64_tr_b16 a[66:67], v28 offset:16160\n"
      "v_mfma_f32_16x16x32_bf16 v[156:159], a[8:11], a[56:59], v[156:159]\n"
      "buffer_load_dwordx4 v39, s[24:27], s29 offen offset:2048 lds\n"
      "v_mfma_f32_16x16x32_bf16 v[188:191], a[12:15], a[56:59], v[188:191]\n"
      "ds_read_b64_tr_b16 a[68:69], v27 offset:14400\n"
      "v_mfma_f32_16x16x32_bf16 v[184:187], a[12:15], a[52:55], v[184:187]\n"
      "v_mfma_f32_16x16x32_bf16 v[180:183], a[12:15], a[48:51], v[180:183]\n"
      "ds_read_b64_tr_b16 a[70:71], v27 offset:16192\n"
      "v_mfma_f32_16x16x32_bf16 v[176:179], a[12:15], a[44:47], v[176:179]\n"
      "buffer_load_dwordx4 v40, s[24:27], s29 offen offset:3072 lds\n"
      "v_mfma_f32_16x16x32_bf16 v[172:175], a[12:15], a[40:43], v[172:175]\n"
      "ds_read_b64_tr_b16 a[72:73], v28 offset:14432\n"
      "v_mfma_f32_16x16x32_bf16 v[168:171], a[12:15], a[36:39], v[168:171]\n"
      "v_mfma_f32_16x16x32_bf16 v[164:167], a[12:15], a[32:35], v[164:167]\n"
      "ds_read_b64_tr_b16 a[74:75], v28 offset:16224\n"
      "s_add_u32 m0, s85, 4096\n"
      "v_mfma_f32_16x16x32_bf16 v[160:163], a[12:15], a[28:31], v[160:163]\n"
      "buffer_load_dwordx4 v41, s[24:27], s29 offen offset:0 lds\n"
      "v_mfma_f32_16x16x32_bf16 v[192:195], a[16:19], a[28:31], v[192:195]\n"
      "ds_read_b64_tr_b16 a[76:77], v27 offset:14464\n"
      "v_mfma_f32_16x16x32_bf16 v[196:199], a[16:19], a[32:35], v[196:199]\n"
      "v_mfma_f32_16x16x32_bf16 v[200:203], a[16:19], a[36:39], v[200:203]\n"
      "ds_read_b64_tr_b16 a[78:79], v27 offset:16256\n"
      "v_mfma_f32_16x16x32_bf16 v[204:207], a[16:19], a[40:43], v[204:207]\n"
      "buffer_load_dwordx4 v42, s[24:27], s29 offen offset:1024 lds\n"
      "v_mfma_f32_16x16x32_bf16 v[208:211], a[16:19], a[44:47], v[208:211]\n"
      "ds_read_b64_tr_b16 a[80:81], v28 offset:14496\n"
      "v_mfma_f32_16x16x32_bf16 v[212:215], a[16:19], a[48:51], v[212:215]\n"
      "v_mfma_f32_16x16x32_bf16 v[216:219], a[16:19], a[52:55], v[216:219]\n"
      "ds_read_b64_tr_b16 a[82:83], v28 offset:16288\n"
      "v_mfma_f32_16x16x32_bf16 v[220:223], a[16:19], a[56:59], v[220:223]\n"
      "buffer_load_dwordx4 v43, s[24:27], s29 offen offset:2048 lds\n"
      "v_mfma_f32_16x16x32_bf16 v[252:255], a[20:23], a[56:59], v[252:255]\n"
      "ds_read_b64_tr_b16 a[84:85], v29 offset:14336\n"
      "v_mfma_f32_16x16x32_bf16 v[248:251], a[20:23], a[52:55], v[248:251]\n"
      "v_mfma_f32_16x16x32_bf16 v[244:247], a[20:23], a[48:51], v[244:247]\n"
      "ds_read_b64_tr_b16 a[86:87], v29 offset:16128\n"
      "v_mfma_f32_16x16x32_bf16 v[240:243], a[20:23], a[44:47], v[240:243]\n"
      "buffer_load_dwordx4 v44, s[24:27], s29 offen offset:3072 lds\n"
      "v_mfma_f32_16x16x32_bf16 v[236:239], a[20:23], a[40:43], v[236:239]\n"
      "v_mfma_f32_16x16x32_bf16 v[232:235], a[20:23], a[36:39], v[232:235]\n"
      "v_mfma_f32_16x16x32_bf16 v[228:231], a[20:23], a[32:35], v[228:231]\n"
      "s_sub_u32 s31, s96, s95\n"
      "v_mfma_f32_16x16x32_bf16 v[224:227], a[20:23], a[28:31], v[224:227]\n"
      "v_add_u32 v25, s31, v25\n"
      "v_mfma_f32_16x16x32_bf16 a[128:131], a[24:27], a[28:31], a[128:131]\n"
      "v_add_u32 v26, s31, v26\n"
      "v_mfma_f32_16x16x32_bf16 a[132:135], a[24:27], a[32:35], a[132:135]\n"
      "s_mov_b32 s31, s95\n"
      "v_mfma_f32_16x16x32_bf16 a[136:139], a[24:27], a[36:39], a[136:139]\n"
      "s_mov_b32 s95, s96\n"
      "v_mfma_f32_16x16x32_bf16 a[140:143], a[24:27], a[40:43], a[140:143]\n"
      "s_mov_b32 s96, s97\n"
      "v_mfma_f32_16x16x32_bf16 a[144:147], a[24:27], a[44:47], a[144:147]\n"
      "s_mov_b32 s97, s31\n"
      "v_mfma_f32_16x16x32_bf16 a[148:151], a[24:27], a[48:51], a[148:151]\n"
      "v_mfma_f32_16x16x32_bf16 a[152:155], a[24:27], a[52:55], a[152:155]\n"
      "v_mfma_f32_16x16x32_bf16 a[156:159], a[24:27], a[56:59], a[156:159]\n"
      "s_waitcnt vmcnt(8) lgkmcnt(0)\n"
      "s_barrier\n"
      "v_cmp_ne_u64 vcc, s[54:55], v[56:57]\n"
      "s_cbranch_vccz Lk_pd_5\n"
      "s_mov_b32 s84, 0x40000\n"
      "Lk_pl_6:\n"
      "buffer_load_dwordx2 v[56:57], v55, s[64:67], s80 offen sc1\n"
      "s_waitcnt vmcnt(0)\n"
      "v_cmp_ne_u64 vcc, s[54:55], v[56:57]\n"
      "s_cbranch_vccz Lk_pd_5\n"
      "s_sleep 1\n"
      "s_sub_u32 s84, s84, 1\n"
      "s_cmp_lg_u32 s84, 0\n"
      "s_cbranch_scc1 Lk_pl_6\n"
      "Lk_pd_5:\n"
      "v_mfma_f32_16x16x32_bf16 v[64:67], a[60:63], a[88:91], v[64:67]\n"
      "ds_read_b64_tr_b16 a[0:1], v30 offset:0\n"
      "v_bfe_u32 v5, v4, 4, 1\n"
      "v_mfma_f32_16x16x32_bf16 v[68:71], a[60:63], a[92:95], v[68:71]\n"
      "s_add_u32 m0, s92, 32768\n"
      "v_bfe_u32 v6, v4, 5, 1\n"
      "v_mfma_f32_16x16x32_bf16 v[72:75], a[60:63], a[96:99], v[72:75]\n"
      "ds_read_b64_tr_b16 a[2:3], v30 offset:1792\n"
      "buffer_load_dwordx4 v33, s[64:67], s28 offen offset:0 sc1 lds\n"
      "v_mfma_f32_16x16x32_bf16 v[76:79], a[60:63], a[100:103], v[76:79]\n"
      "v_and_b32 v7, 15, v4\n"
      "v_lshlrev_b32 v7, 4, v7\n"
      "v_mfma_f32_16x16x32_bf16 v[80:83], a[60:63], a[104:107], v[80:83]\n"
      "ds_read_b128 a[28:31], v25 offset:0\n"
      "v_lshl_add_u32 v7, v6, 8, v7\n"
      "v_mfma_f32_16x16x32_bf16 v[84:87], a[60:63], a[108:111], v[84:87]\n"
      "v_lshl_add_u32 v8, v5, 9, v7\n"
      "v_add_u32 v9, 4096, v8\n"
      "v_mfma_f32_16x16x32_bf16 v[88:91], a[60:63], a[112:115], v[88:91]\n"
      "ds_read_b128 a[32:35], v25 offset:2048\n"
      "v_add_u32 v10, 8192, v8\n"
      "v_mfma_f32_16x16x32_bf16 v[92:95], a[60:63], a[116:119], v[92:95]\n"
      "buffer_load_dwordx4 v34, s[64:67], s28 offen offset:1024 sc1 lds\n"
      "v_add_u32 v11, 12288, v8\n"
      "v_mfma_f32_16x16x32_bf16 v[124:127], a[64:67], a[116:119], v[124:127]\n"
      "ds_read_b128 a[36:39], v25 offset:4096\n"
      "v_lshl_add_u32 v12, v5, 11, v7\n"
      "v_mfma_f32_16x16x32_bf16 v[120:123], a[64:67], a[112:115], v[120:123]\n"
      "v_add_u32 v13, 4096, v12\n"
      "v_add_u32 v14, 8192, v12\n"
      "v_mfma_f32_16x16x32_bf16 v[116:119], a[64:67], a[108:111], v[116:119]\n"
      "ds_read_b128 a[40:43], v25 offset:6144\n"
      "v_add_u32 v15, 12288, v12\n"
      "v_mfma_f32_16x16x32_bf16 v[112:115], a[64:67], a[104:107], v[112:115]\n"
      "s_mul_i32 s76, s59, 0x1c0000\n"
      "s_lshl_b32 s77, s63, 14\n"
      "v_mfma_f32_16x16x32_bf16 v[108:111], a[64:67], a[100:103], v[108:111]\n"
      "ds_read_b128 a[44:47], v25 offset:8192\n"
      "buffer_load_dwordx4 v35, s[64:67], s28 offen offset:2048 sc1 lds\n"
      "v_mfma_f32_16x16x32_bf16 v[104:107], a[64:67], a[96:99], v[104:107]\n"
      "s_add_u32 s76, s76, s77\n"
      "s_add_u32 s76, s76, 0x1000000\n"
      "v_mfma_f32_16x16x32_bf16 v[100:103], a[64:67], a[92:95], v[100:103]\n"
      "ds_read_b128 a[48:51], v25 offset:10240\n"
      "s_mul_i32 s77, s60, 7\n"
      "v_mfma_f32_16x16x32_bf16 v[96:99], a[64:67], a[88:91], v[96:99]\n"
      "s_lshl_b32 s78, s62, 2\n"
      "s_add_u32 s79, s77, s78\n"
      "v_mfma_f32_16x16x32_bf16 v[128:131], a[68:71], a[88:91], v[128:131]\n"
      "ds_read_b128 a[52:55], v25 offset:12288\n"
      "s_add_u32 s79, s79, 0\n"
      "v_mfma_f32_16x16x32_bf16 v[132:135], a[68:71], a[92:95], v[132:135]\n"
      "buffer_load_dwordx4 v36, s[64:67], s28 offen offset:3072 sc1 lds\n"
      "s_lshr_b32 s72, s79, 1\n"
      "v_mfma_f32_16x16x32_bf16 v[136:139], a[68:71], a[96:99], v[136:139]\n"
      "ds_read_b128 a[56:59], v25 offset:14336\n"
      "s_and_b32 s83, s10, 15\n"
      "s_lshr_b64 s[10:11], s[10:11], 4\n"
      "v_mfma_f32_16x16x32_bf16 v[140:143], a[68:71], a[100:103], v[140:143]\n"
      "s_mul_i32 s28, s83, s30\n"
      "s_add_u32 s28, s28, s81\n"
      "s_lshl_b32 s29, s83, 7\n"
      "s_add_u32 s29, s29, s82\n"
      "v_mfma_f32_16x16x32_bf16 v[144:147], a[68:71], a[104:107], v[144:147]\n"
      "ds_read_b64_tr_b16 a[4:5], v31 offset:32\n"
      "s_lshl_b32 s72, s72, 15\n"
      "v_mfma_f32_16x16x32_bf16 v[148:151], a[68:71], a[108:111], v[148:151]\n"
      "s_add_u32 m0, s92, 81920\n"
      "s_and_b32 s79, s79, 1\n"
      "v_mfma_f32_16x16x32_bf16 v[152:155], a[68:71], a[112:115], v[152:155]\n"
      "ds_read_b64_tr_b16 a[6:7], v31 offset:1824\n"
      "buffer_load_dwordx4 v33, s[64:67], s28 offen offset:0 sc1 lds\n"
      "v_mfma_f32_16x16x32_bf16 v[156:159], a[68:71], a[116:119], v[156:159]\n"
      "s_lshl_b32 s79, s79, 10\n"
      "s_add_u32 s72, s72, s79\n"
      "v_mfma_f32_16x16x32_bf16 v[188:191], a[72:75], a[116:119], v[188:191]\n"
      "ds_read_b64_tr_b16 a[8:9], v30 offset:64\n"
      "s_add_u32 s72, s72, s76\n"
      "v_mfma_f32_16x16x32_bf16 v[184:187], a[72:75], a[112:115], v[184:187]\n"
      "s_add_u32 s79, s77, s78\n"
      "s_add_u32 s79, s79, 1\n"
      "v_mfma_f32_16x16x32_bf16 v[180:183], a[72:75], a[108:111], v[180:183]\n"
      "ds_read_b64_tr_b16 a[10:11], v30 offset:1856\n"
      "s_lshr_b32 s73, s79, 1\n"
      "v_mfma_f32_16x16x32_bf16 v[176:179], a[72:75], a[104:107], v[176:179]\n"
      "s_lshl_b32 s73, s73, 15\n"
      "s_and_b32 s79, s79, 1\n"
      "v_mfma_f32_16x16x32_bf16 v[172:175], a[72:75], a[100:103], v[172:175]\n"
      "ds_read_b64_tr_b16 a[12:13], v31 offset:96\n"
      "buffer_load_dwordx4 v34, s[64:67], s28 offen offset:1024 sc1 lds\n"
      "v_mfma_f32_16x16x32_bf16 v[168:171], a[72:75], a[96:99], v[168:171]\n"
      "s_lshl_b32 s79, s79, 10\n"
      "s_add_u32 s73, s73, s79\n"
      "v_mfma_f32_16x16x32_bf16 v[164:167], a[72:75], a[92:95], v[164:167]\n"
      "ds_read_b64_tr_b16 a[14:15], v31 offset:1888\n"
      "s_add_u32 s73, s73, s76\n"
      "v_mfma_f32_16x16x32_bf16 v[160:163], a[72:75], a[88:91], v[160:163]\n"
      "s_add_u32 s79, s77, s78\n"
      "s_add_u32 s79, s79, 2\n"
      "v_mfma_f32_16x16x32_bf16 v[192:195], a[76:79], a[88:91], v[192:195]\n"
      "ds_read_b64_tr_b16 a[16:17], v30 offset:128\n"
      "s_lshr_b32 s74, s79, 1\n"
      "v_mfma_f32_16x16x32_bf16 v[196:199], a[76:79], a[92:95], v[196:199]\n"
      "buffer_load_dwordx4 v35, s[64:67], s28 offen offset:2048 sc1 lds\n"
      "s_lshl_b32 s74, s74, 15\n"
      "v_mfma_f32_16x16x32_bf16 v[200:203], a[76:79], a[96:99], v[200:203]\n"
      "ds_read_b64_tr_b16 a[18:19], v30 offset:1920\n"
      "s_and_b32 s79, s79, 1\n"
      "v_mfma_f32_16x16x32_bf16 v[204:207], a[76:79], a[100:103], v[204:207]\n"
      "s_lshl_b32 s79, s79, 10\n"
      "s_add_u32 s74, s74, s79\n"
      "v_mfma_f32_16x16x32_bf16 v[208:211], a[76:79], a[104:107], v[208:211]\n"
      "ds_read_b64_tr_b16 a[20:21], v31 offset:160\n"
      "s_add_u32 s74, s74, s76\n"
      "v_mfma_f32_16x16x32_bf16 v[212:215], a[76:79], a[108:111], v[212:215]\n"
      "s_add_u32 s79, s77, 3\n"
      "s_lshr_b32 s75, s79, 1\n"
      "v_mfma_f32_16x16x32_bf16 v[216:219], a[76:79], a[112:115], v[216:219]\n"
      "ds_read_b64_tr_b16 a[22:23], v31 offset:1952\n"
      "buffer_load_dwordx4 v36, s[64:67], s28 offen offset:3072 sc1 lds\n"
      "v_mfma_f32_16x16x32_bf16 v[220:223], a[76:79], a[116:119], v[220:223]\n"
      "s_and_b32 s83, s10, 15\n"
      "s_lshr_b64 s[10:11], s[10:11], 4\n"
      "v_mfma_f32_16x16x32_bf16 v[252:255], a[80:83], a[116:119], v[252:255]\n"
      "ds_read_b64_tr_b16 a[24:25], v32 offset:0\n"
      "s_mul_i32 s28, s83, s30\n"
      "s_add_u32 s28, s28, s81\n"
      "v_mfma_f32_16x16x32_bf16 v[248:251], a[80:83], a[112:115], v[248:251]\n"
      "s_lshl_b32 s75, s75, 15\n"
      "s_and_b32 s79, s79, 1\n"
      "v_mfma_f32_16x16x32_bf16 v[244:247], a[80:83], a[108:111], v[244:247]\n"
      "ds_read_b64_tr_b16 a[26:27], v32 offset:1792\n"
      "s_add_u32 m0, s85, 49152\n"
      "v_mfma_f32_16x16x32_bf16 v[240:243], a[80:83], a[104:107], v[240:243]\n"
      "buffer_load_dwordx4 v37, s[24:27], s29 offen offset:0 lds\n"
      "s_lshl_b32 s79, s79, 10\n"
      "v_mfma_f32_16x16x32_bf16 v[236:239], a[80:83], a[100:103], v[236:239]\n"
      "s_add_u32 s75, s75, s79\n"
      "s_add_u32 s75, s75, s76\n"
      "v_mfma_f32_16x16x32_bf16 v[232:235], a[80:83], a[96:99], v[232:235]\n"
      "s_lshl_b32 s79, s62, 9\n"
      "s_add_u32 s75, s75, s79\n"
      "v_mfma_f32_16x16x32_bf16 v[228:231], a[80:83], a[92:95], v[228:231]\n"
      "v_mfma_f32_16x16x32_bf16 v[224:227], a[80:83], a[88:91], v[224:227]\n"
      "v_mfma_f32_16x16x32_bf16 a[128:131], a[84:87], a[88:91], a[128:131]\n"
      "buffer_load_dwordx4 v38, s[24:27], s29 offen offset:1024 lds\n"
      "v_mfma_f32_16x16x32_bf16 a[132:135], a[84:87], a[92:95], a[132:135]\n"
      "v_mfma_f32_16x16x32_bf16 a[136:139], a[84:87], a[96:99], a[136:139]\n"
      "v_mfma_f32_16x16x32_bf16 a[140:143], a[84:87], a[100:103], a[140:143]\n"
      "v_mfma_f32_16x16x32_bf16 a[144:147], a[84:87], a[104:107], a[144:147]\n"
      "v_mfma_f32_16x16x32_bf16 a[148:151], a[84:87], a[108:111], a[148:151]\n"
      "v_mfma_f32_16x16x32_bf16 a[152:155], a[84:87], a[112:115], a[152:155]\n"
      "v_mfma_f32_16x16x32_bf16 a[156:159], a[84:87], a[116:119], a[156:159]\n"
      "s_waitcnt lgkmcnt(0)\n"
      "v_mfma_f32_16x16x32_bf16 v[64:67], a[0:3], a[28:31], v[64:67]\n"
      "ds_read_b64_tr_b16 a[60:61], v30 offset:14336\n"
      "v_mfma_f32_16x16x32_bf16 v[68:71], a[0:3], a[32:35], v[68:71]\n"
      "ds_read_b64_tr_b16 a[62:63], v30 offset:16128\n"
      "buffer_load_dwordx4 v39, s[24:27], s29 offen offset:2048 lds\n"
      "v_mfma_f32_16x16x32_bf16 v[72:75], a[0:3], a[36:39], v[72:75]\n"
      "ds_read_b128 a[88:91], v26 offset:0\n"
      "v_mfma_f32_16x16x32_bf16 v[76:79], a[0:3], a[40:43], v[76:79]\n"
      "ds_read_b128 a[92:95], v26 offset:2048\n"
      "v_mfma_f32_16x16x32_bf16 v[80:83], a[0:3], a[44:47], v[80:83]\n"
      "ds_read_b128 a[96:99], v26 offset:4096\n"
      "v_mfma_f32_16x16x32_bf16 v[84:87], a[0:3], a[48:51], v[84:87]\n"
      "ds_read_b128 a[100:103], v26 offset:6144\n"
      "v_mfma_f32_16x16x32_bf16 v[88:91], a[0:3], a[52:55], v[88:91]\n"
      "ds_read_b128 a[104:107], v26 offset:8192\n"
      "buffer_load_dwordx4 v40, s[24:27], s29 offen offset:3072 lds\n"
      "v_mfma_f32_16x16x32_bf16 v[92:95], a[0:3], a[56:59], v[92:95]\n"
      "ds_read_b128 a[108:111], v26 offset:10240\n"
      "v_mfma_f32_16x16x32_bf16 v[124:127], a[4:7], a[56:59], v[124:127]\n"
      "ds_read_b128 a[112:115], v26 offset:12288\n"
      "v_mfma_f32_16x16x32_bf16 v[120:123], a[4:7], a[52:55], v[120:123]\n"
      "ds_read_b128 a[116:119], v26 offset:14336\n"
      "v_mfma_f32_16x16x32_bf16 v[116:119], a[4:7], a[48:51], v[116:119]\n"
      "ds_read_b64_tr_b16 a[64:65], v31 offset:14368\n"
      "s_add_u32 m0, s85, 53248\n"
      "v_mfma_f32_16x16x32_bf16 v[112:115], a[4:7], a[44:47], v[112:115]\n"
      "buffer_load_dwordx4 v41, s[24:27], s29 offen offset:0 lds\n"
      "v_mfma_f32_16x16x32_bf16 v[108:111], a[4:7], a[40:43], v[108:111]\n"
      "ds_read_b64_tr_b16 a[66:67], v31 offset:16160\n"
      "v_mfma_f32_16x16x32_bf16 v[104:107], a[4:7], a[36:39], v[104:107]\n"
      "ds_read_b64_tr_b16 a[68:69], v30 offset:14400\n"
      "v_mfma_f32_16x16x32_bf16 v[100:103], a[4:7], a[32:35], v[100:103]\n"
      "ds_read_b64_tr_b16 a[70:71], v30 offset:16192\n"
      "v_mfma_f32_16x16x32_bf16 v[96:99], a[4:7], a[28:31], v[96:99]\n"
      "ds_read_b64_tr_b16 a[72:73], v31 offset:14432\n"
      "v_mfma_f32_16x16x32_bf16 v[128:131], a[8:11], a[28:31], v[128:131]\n"
      "ds_read_b64_tr_b16 a[74:75], v31 offset:16224\n"
      "buffer_load_dwordx4 v42, s[24:27], s29 offen offset:1024 lds\n"
      "v_mfma_f32_16x16x32_bf16 v[132:135], a[8:11], a[32:35], v[132:135]\n"
      "ds_read_b64_tr_b16 a[76:77], v30 offset:14464\n"
      "v_mfma_f32_16x16x32_bf16 v[136:139], a[8:11], a[36:39], v[136:139]\n"
      "ds_read_b64_tr_b16 a[78:79], v30 offset:16256\n"
      "v_mfma_f32_16x16x32_bf16 v[140:143], a[8:11], a[40:43], v[140:143]\n"
      "ds_read_b64_tr_b16 a[80:81], v31 offset:14496\n"
      "v_mfma_f32_16x16x32_bf16 v[144:147], a[8:11], a[44:47], v[144:147]\n"
      "ds_read_b64_tr_b16 a[82:83], v31 offset:16288\n"
      "v_mfma_f32_16x16x32_bf16 v[148:151], a[8:11], a[48:51], v[148:151]\n"
      "ds_read_b64_tr_b16 a[84:85], v32 offset:14336\n"
      "buffer_load_dwordx4 v43, s[24:27], s29 offen offset:2048 lds\n"
      "v_mfma_f32_16x16x32_bf16 v[152:155], a[8:11], a[52:55], v[152:155]\n"
      "ds_read_b64_tr_b16 a[86:87], v32 offset:16128\n"
      "v_mfma_f32_16x16x32_bf16 v[156:159], a[8:11], a[56:59], v[156:159]\n"
      "v_mfma_f32_16x16x32_bf16 v[188:191], a[12:15], a[56:59], v[188:191]\n"
      "v_mfma_f32_16x16x32_bf16 v[184:187], a[12:15], a[52:55], v[184:187]\n"
      "v_mfma_f32_16x16x32_bf16 v[180:183], a[12:15], a[48:51], v[180:183]\n"
      "buffer_load_dwordx4 v44, s[24:27], s29 offen offset:3072 lds\n"
      "s_lshl_b32 s29, s83, 7\n"
      "s_add_u32 s29, s29, s82\n"
      "v_mfma_f32_16x16x32_bf16 v[176:179], a[12:15], a[44:47], v[176:179]\n"
      "v_mfma_f32_16x16x32_bf16 v[172:175], a[12:15], a[40:43], v[172:175]\n"
      "v_mfma_f32_16x16x32_bf16 v[168:171], a[12:15], a[36:39], v[168:171]\n"
      "v_mfma_f32_16x16x32_bf16 v[164:167], a[12:15], a[32:35], v[164:167]\n"
      "v_mfma_f32_16x16x32_bf16 v[160:163], a[12:15], a[28:31], v[160:163]\n"
      "s_waitcnt lgkmcnt(0)\n"
      "s_barrier\n"
      "v_mfma_f32_16x16x32_bf16 v[64:67], a[60:63], a[88:91], v[64:67]\n"
      "s_add_u32 m0, s92, 131072\n"
      "v_mfma_f32_16x16x32_bf16 v[96:99], a[64:67], a[88:91], v[96:99]\n"
      "buffer_load_dwordx4 v33, s[64:67], s28 offen offset:0 sc1 lds\n"
      "v_mfma_f32_16x16x32_bf16 v[100:103], a[64:67], a[92:95], v[100:103]\n"
      "v_mfma_f32_16x16x32_bf16 v[68:71], a[60:63], a[92:95], v[68:71]\n"
      "v_mfma_f32_16x16x32_bf16 v[72:75], a[60:63], a[96:99], v[72:75]\n"
      "v_mfma_f32_16x16x32_bf16 v[104:107], a[64:67], a[96:99], v[104:107]\n"
      "buffer_load_dwordx4 v34, s[64:67], s28 offen offset:1024 sc1 lds\n"
      "v_mfma_f32_16x16x32_bf16 v[108:111], a[64:67], a[100:103], v[108:111]\n"
      "v_mfma_f32_16x16x32_bf16 v[76:79], a[60:63], a[100:103], v[76:79]\n"
      "v_mfma_f32_16x16x32_bf16 v[80:83], a[60:63], a[104:107], v[80:83]\n"
      "v_mfma_f32_16x16x32_bf16 v[112:115], a[64:67], a[104:107], v[112:115]\n"
      "v_mfma_f32_16x16x32_bf16 v[116:119], a[64:67], a[108:111], v[116:119]\n"
      "buffer_load_dwordx4 v35, s[64:67], s28 offen offset:2048 sc1 lds\n"
      "v_cvt_pk_bf16_f32 v64, v64, v65\n"
      "v_cvt_pk_bf16_f32 v65, v66, v67\n"
      "v_mfma_f32_16x16x32_bf16 v[84:87], a[60:63], a[108:111], v[84:87]\n"
      "v_cvt_pk_bf16_f32 v66, v96, v97\n"
      "v_cvt_pk_bf16_f32 v67, v98, v99\n"
      "v_pk_max_i16 v64, v64, 0\n"
      "v_mfma_f32_16x16x32_bf16 v[88:91], a[60:63], a[112:115], v[88:91]\n"
      "v_pk_max_i16 v66, v66, 0\n"
      "v_pk_max_i16 v65, v65, 0\n"
      "v_pk_max_i16 v67, v67, 0\n"
      "v_mfma_f32_16x16x32_bf16 v[120:123], a[64:67], a[112:115], v[120:123]\n"
      "v_permlane16_swap_b32 v64, v66\n"
      "s_nop 0\n"
      "v_permlane16_swap_b32 v65, v67\n"
      "v_mfma_f32_16x16x32_bf16 v[124:127], a[64:67], a[116:119], v[124:127]\n"
      "buffer_load_dwordx4 v36, s[64:67], s28 offen offset:3072 sc1 lds\n"
      "v_mfma_f32_16x16x32_bf16 v[92:95], a[60:63], a[116:119], v[92:95]\n"
      "s_and_b32 s83, s10, 15\n"
      "s_lshr_b64 s[10:11], s[10:11], 4\n"
      "buffer_store_dwordx4 v[64:67], v8, s[64:67], s72 offen offset:0 sc1\n"
      "v_mfma_f32_16x16x32_bf16 v[128:131], a[68:71], a[88:91], v[128:131]\n"
      "s_mul_i32 s28, s83, s30\n"
      "s_add_u32 s28, s28, s81\n"
      "v_cvt_pk_bf16_f32 v68, v68, v69\n"
      "v_mfma_f32_16x16x32_bf16 v[160:163], a[72:75], a[88:91], v[160:163]\n"
      "v_cvt_pk_bf16_f32 v69, v70, v71\n"
      "v_cvt_pk_bf16_f32 v70, v100, v101\n"
      "v_cvt_pk_bf16_f32 v71, v102, v103\n"
      "v_mfma_f32_16x16x32_bf16 v[164:167], a[72:75], a[92:95], v[164:167]\n"
      "s_add_u32 m0, s85, 98304\n"
      "v_pk_max_i16 v68, v68, 0\n"
      "v_pk_max_i16 v70, v70, 0\n"
      "v_mfma_f32_16x16x32_bf16 v[132:135], a[68:71], a[92:95], v[132:135]\n"
      "buffer_load_dwordx4 v37, s[24:27], s29 offen offset:0 lds\n"
      "v_pk_max_i16 v69, v69, 0\n"
      "v_pk_max_i16 v71, v71, 0\n"
      "v_mfma_f32_16x16x32_bf16 v[136:139], a[68:71], a[96:99], v[136:139]\n"
      "v_permlane16_swap_b32 v68, v70\n"
      "s_nop 0\n"
      "v_permlane16_swap_b32 v69, v71\n"
      "v_mfma_f32_16x16x32_bf16 v[168:171], a[72:75], a[96:99], v[168:171]\n"
      "buffer_store_dwordx4 v[68:71], v8, s[64:67], s72 offen offset:2048 sc1\n"
      "v_cvt_pk_bf16_f32 v72, v72, v73\n"
      "v_cvt_pk_bf16_f32 v73, v74, v75\n"
      "v_mfma_f32_16x16x32_bf16 v[172:175], a[72:75], a[100:103], v[172:175]\n"
      "v_cvt_pk_bf16_f32 v74, v104, v105\n"
      "v_cvt_pk_bf16_f32 v75, v106, v107\n"
      "v_pk_max_i16 v72, v72, 0\n"
      "v_mfma_f32_16x16x32_bf16 v[140:143], a[68:71], a[100:103], v[140:143]\n"
      "buffer_load_dwordx4 v38, s[24:27], s29 offen offset:1024 lds\n"
      "v_pk_max_i16 v74, v74, 0\n"
      "v_pk_max_i16 v73, v73, 0\n"
      "v_mfma_f32_16x16x32_bf16 v[144:147], a[68:71], a[104:107], v[144:147]\n"
      "v_pk_max_i16 v75, v75, 0\n"
      "v_permlane16_swap_b32 v72, v74\n"
      "s_nop 0\n"
      "v_mfma_f32_16x16x32_bf16 v[176:179], a[72:75], a[104:107], v[176:179]\n"
      "v_permlane16_swap_b32 v73, v75\n"
      "v_mfma_f32_16x16x32_bf16 v[180:183], a[72:75], a[108:111], v[180:183]\n"
      "v_mfma_f32_16x16x32_bf16 v[148:151], a[68:71], a[108:111], v[148:151]\n"
      "buffer_store_dwordx4 v[72:75], v9, s[64:67], s72 offen offset:0 sc1\n"
      "v_cvt_pk_bf16_f32 v76, v76, v77\n"
      "v_cvt_pk_bf16_f32 v77, v78, v79\n"
      "v_mfma_f32_16x16x32_bf16 v[152:155], a[68:71], a[112:115], v[152:155]\n"
      "buffer_load_dwordx4 v39, s[24:27], s29 offen offset:2048 lds\n"
      "v_cvt_pk_bf16_f32 v78, v108, v109\n"
      "v_cvt_pk_bf16_f32 v79, v110, v111\n"
      "v_mfma_f32_16x16x32_bf16 v[184:187], a[72:75], a[112:115], v[184:187]\n"
      "v_pk_max_i16 v76, v76, 0\n"
      "v_pk_max_i16 v78, v78, 0\n"
      "v_pk_max_i16 v77, v77, 0\n"
      "v_mfma_f32_16x16x32_bf16 v[188:191], a[72:75], a[116:119], v[188:191]\n"
      "v_pk_max_i16 v79, v79, 0\n"
      "v_permlane16_swap_b32 v76, v78\n"
      "s_nop 0\n"
      "v_mfma_f32_16x16x32_bf16 v[156:159], a[68:71], a[116:119], v[156:159]\n"
      "v_permlane16_swap_b32 v77, v79\n"
      "v_mfma_f32_16x16x32_bf16 v[192:195], a[16:19], a[28:31], v[192:195]\n"
      "buffer_load_dwordx4 v40, s[24:27], s29 offen offset:3072 lds\n"
      "v_mfma_f32_16x16x32_bf16 v[196:199], a[16:19], a[32:35], v[196:199]\n"
      "buffer_store_dwordx4 v[76:79], v9, s[64:67], s72 offen offset:2048 sc1\n"
      "v_cvt_pk_bf16_f32 v80, v80, v81\n"
      "v_cvt_pk_bf16_f32 v81, v82, v83\n"
      "v_mfma_f32_16x16x32_bf16 v[200:203], a[16:19], a[36:39], v[200:203]\n"
      "v_cvt_pk_bf16_f32 v82, v112, v113\n"
      "v_cvt_pk_bf16_f32 v83, v114, v115\n"
      "v_pk_max_i16 v80, v80, 0\n"
      "v_mfma_f32_16x16x32_bf16 v[204:207], a[16:19], a[40:43], v[204:207]\n"
      "v_pk_max_i16 v82, v82, 0\n"
      "v_pk_max_i16 v81, v81, 0\n"
      "v_pk_max_i16 v83, v83, 0\n"
      "v_mfma_f32_16x16x32_bf16 v[208:211], a[16:19], a[44:47], v[208:211]\n"
      "s_add_u32 m0, s85, 102400\n"
      "v_permlane16_swap_b32 v80, v82\n"
      "s_nop 0\n"
      "v_mfma_f32_16x16x32_bf16 v[212:215], a[16:19], a[48:51], v[212:215]\n"
      "buffer_load_dwordx4 v41, s[24:27], s29 offen offset:0 lds\n"
      "v_permlane16_swap_b32 v81, v83\n"
      "v_mfma_f32_16x16x32_bf16 v[216:219], a[16:19], a[52:55], v[216:219]\n"
      "v_mfma_f32_16x16x32_bf16 v[220:223], a[16:19], a[56:59], v[220:223]\n"
      "buffer_store_dwordx4 v[80:83], v10, s[64:67], s72 offen offset:0 sc1\n"
      "v_cvt_pk_bf16_f32 v84, v84, v85\n"
      "v_cvt_pk_bf16_f32 v85, v86, v87\n"
      "v_mfma_f32_16x16x32_bf16 v[252:255], a[20:23], a[56:59], v[252:255]\n"
      "v_cvt_pk_bf16_f32 v86, v116, v117\n"
      "v_cvt_pk_bf16_f32 v87, v118, v119\n"
      "v_pk_max_i16 v84, v84, 0\n"
      "v_mfma_f32_16x16x32_bf16 v[248:251], a[20:23], a[52:55], v[248:251]\n"
      "buffer_load_dwordx4 v42, s[24:27], s29 offen offset:1024 lds\n"
      "v_pk_max_i16 v86, v86, 0\n"
      "v_pk_max_i16 v85, v85, 0\n"
      "v_mfma_f32_16x16x32_bf16 v[244:247], a[20:23], a[48:51], v[244:247]\n"
      "v_pk_max_i16 v87, v87, 0\n"
      "v_permlane16_swap_b32 v84, v86\n"
      "s_nop 0\n"
      "v_mfma_f32_16x16x32_bf16 v[240:243], a[20:23], a[44:47], v[240:243]\n"
      "v_permlane16_swap_b32 v85, v87\n"
      "v_mfma_f32_16x16x32_bf16 v[236:239], a[20:23], a[40:43], v[236:239]\n"
      "v_mfma_f32_16x16x32_bf16 v[232:235], a[20:23], a[36:39], v[232:235]\n"
      "buffer_store_dwordx4 v[84:87], v10, s[64:67], s72 offen offset:2048 sc1\n"
      "v_cvt_pk_bf16_f32 v88, v88, v89\n"
      "v_cvt_pk_bf16_f32 v89, v90, v91\n"
      "v_mfma_f32_16x16x32_bf16 v[228:231], a[20:23], a[32:35], v[228:231]\n"
      "buffer_load_dwordx4 v43, s[24:27], s29 offen offset:2048 lds\n"
      "v_cvt_pk_bf16_f32 v90, v120, v121\n"
      "v_cvt_pk_bf16_f32 v91, v122, v123\n"
      "v_mfma_f32_16x16x32_bf16 v[224:227], a[20:23], a[28:31], v[224:227]\n"
      "v_pk_max_i16 v88, v88, 0\n"
      "v_pk_max_i16 v90, v90, 0\n"
      "v_pk_max_i16 v89, v89, 0\n"
      "v_mfma_f32_16x16x32_bf16 a[128:131], a[24:27], a[28:31], a[128:131]\n"
      "v_pk_max_i16 v91, v91, 0\n"
      "v_permlane16_swap_b32 v88, v90\n"
      "s_nop 0\n"
      "v_mfma_f32_16x16x32_bf16 a[132:135], a[24:27], a[32:35], a[132:135]\n"
      "v_permlane16_swap_b32 v89, v91\n"
      "v_mfma_f32_16x16x32_bf16 a[136:139], a[24:27], a[36:39], a[136:139]\n"
      "buffer_load_dwordx4 v44, s[24:27], s29 offen offset:3072 lds\n"
      "s_lshl_b32 s29, s83, 7\n"
      "s_add_u32 s29, s29, s82\n"
      "v_mfma_f32_16x16x32_bf16 a[140:143], a[24:27], a[40:43], a[140:143]\n"
      "buffer_store_dwordx4 v[88:91], v11, s[64:67], s72 offen offset:0 sc1\n"
      "v_cvt_pk_bf16_f32 v92, v92, v93\n"
      "v_cvt_pk_bf16_f32 v93, v94, v95\n"
      "v_mfma_f32_16x16x32_bf16 a[144:147], a[24:27], a[44:47], a[144:147]\n"
      "v_cvt_pk_bf16_f32 v94, v124, v125\n"
      "v_cvt_pk_bf16_f32 v95, v126, v127\n"
      "v_pk_max_i16 v92, v92, 0\n"
      "v_mfma_f32_16x16x32_bf16 a[148:151], a[24:27], a[48:51], a[148:151]\n"
      "v_pk_max_i16 v94, v94, 0\n"
      "v_pk_max_i16 v93, v93, 0\n"
      "v_pk_max_i16 v95, v95, 0\n"
      "v_mfma_f32_16x16x32_bf16 a[152:155], a[24:27], a[52:55], a[152:155]\n"
      "v_permlane16_swap_b32 v92, v94\n"
      "s_nop 0\n"
      "v_permlane16_swap_b32 v93, v95\n"
      "v_mfma_f32_16x16x32_bf16 a[156:159], a[24:27], a[56:59], a[156:159]\n"
      "s_waitcnt vmcnt(31)\n"
      "s_barrier\n"
      "v_mfma_f32_16x16x32_bf16 v[192:195], a[76:79], a[88:91], v[192:195]\n"
      "ds_read_b128 a[0:3], v45 offset:32768\n"
      "buffer_store_dwordx4 v[92:95], v11, s[64:67], s72 offen offset:2048 sc1\n"
      "v_cvt_pk_bf16_f32 v128, v128, v129\n"
      "v_mfma_f32_16x16x32_bf16 v[196:199], a[76:79], a[92:95], v[196:199]\n"
      "ds_read_b128 a[16:19], v47 offset:0\n"
      "v_cvt_pk_bf16_f32 v129, v130, v131\n"
      "v_cvt_pk_bf16_f32 v130, v160, v161\n"
      "v_mfma_f32_16x16x32_bf16 v[200:203], a[76:79], a[96:99], v[200:203]\n"
      "ds_read_b128 a[20:23], v47 offset:2048\n"
      "v_cvt_pk_bf16_f32 v131, v162, v163\n"
      "v_pk_max_i16 v128, v128, 0\n"
      "v_mfma_f32_16x16x32_bf16 v[204:207], a[76:79], a[100:103], v[204:207]\n"
      "ds_read_b128 a[24:27], v47 offset:4096\n"
      "v_pk_max_i16 v130, v130, 0\n"
      "v_pk_max_i16 v129, v129, 0\n"
      "v_mfma_f32_16x16x32_bf16 v[208:211], a[76:79], a[104:107], v[208:211]\n"
      "ds_read_b128 a[28:31], v47 offset:6144\n"
      "v_pk_max_i16 v131, v131, 0\n"
      "v_permlane16_swap_b32 v128, v130\n"
      "v_mfma_f32_16x16x32_bf16 v[212:215], a[76:79], a[108:111], v[212:215]\n"
      "ds_read_b128 a[32:35], v47 offset:8192\n"
      "s_nop 0\n"
      "v_permlane16_swap_b32 v129, v131\n"
      "v_mfma_f32_16x16x32_bf16 v[216:219], a[76:79], a[112:115], v[216:219]\n"
      "v_accvgpr_write_b32 a160, v128\n"
      "v_accvgpr_write_b32 a161, v129\n"
      "v_accvgpr_write_b32 a162, v130\n"
      "v_mfma_f32_16x16x32_bf16 v[220:223], a[76:79], a[116:119], v[220:223]\n"
      "ds_read_b128 a[36:39], v47 offset:10240\n"
      "v_accvgpr_write_b32 a163, v131\n"
      "v_cvt_pk_bf16_f32 v132, v132, v133\n"
      "v_mfma_f32_16x16x32_bf16 v[252:255], a[80:83], a[116:119], v[252:255]\n"
      "ds_read_b128 a[40:43], v47 offset:12288\n"
      "v_cvt_pk_bf16_f32 v133, v134, v135\n"
      "v_cvt_pk_bf16_f32 v134, v164, v165\n"
      "v_mfma_f32_16x16x32_bf16 v[248:251], a[80:83], a[112:115], v[248:251]\n"
      "ds_read_b128 a[44:47], v47 offset:14336\n"
      "v_cvt_pk_bf16_f32 v135, v166, v167\n"
      "v_pk_max_i16 v132, v132, 0\n"
      "v_mfma_f32_16x16x32_bf16 v[244:247], a[80:83], a[108:111], v[244:247]\n"
      "ds_read_b128 a[4:7], v45 offset:34816\n"
      "v_pk_max_i16 v134, v134, 0\n"
      "v_pk_max_i16 v133, v133, 0\n"
      "v_mfma_f32_16x16x32_bf16 v[240:243], a[80:83], a[104:107], v[240:243]\n"
      "ds_read_b128 a[8:11], v45 offset:36864\n"
      "v_pk_max_i16 v135, v135, 0\n"
      "v_permlane16_swap_b32 v132, v134\n"
      "v_mfma_f32_16x16x32_bf16 v[236:239], a[80:83], a[100:103], v[236:239]\n"
      "ds_read_b128 a[12:15], v45 offset:38912\n"
      "s_nop 0\n"
      "v_permlane16_swap_b32 v133, v135\n"
      "v_mfma_f32_16x16x32_bf16 v[232:235], a[80:83], a[96:99], v[232:235]\n"
      "v_accvgpr_write_b32 a164, v132\n"
      "v_accvgpr_write_b32 a165, v133\n"
      "v_accvgpr_write_b32 a166, v134\n"
      "v_mfma_f32_16x16x32_bf16 v[228:231], a[80:83], a[92:95], v[228:231]\n"
      "v_accvgpr_write_b32 a167, v135\n"
      "v_cvt_pk_bf16_f32 v136, v136, v137\n"
      "v_cvt_pk_bf16_f32 v137, v138, v139\n"
      "v_mfma_f32_16x16x32_bf16 v[224:227], a[80:83], a[88:91], v[224:227]\n"
      "v_cvt_pk_bf16_f32 v138, v168, v169\n"
      "v_cvt_pk_bf16_f32 v139, v170, v171\n"
      "v_pk_max_i16 v136, v136, 0\n"
      "v_mfma_f32_16x16x32_bf16 a[128:131], a[84:87], a[88:91], a[128:131]\n"
      "v_pk_max_i16 v138, v138, 0\n"
      "v_pk_max_i16 v137, v137, 0\n"
      "v_pk_max_i16 v139, v139, 0\n"
      "v_mfma_f32_16x16x32_bf16 a[132:135], a[84:87], a[92:95], a[132:135]\n"
      "v_permlane16_swap_b32 v136, v138\n"
      "s_nop 0\n"
      "v_permlane16_swap_b32 v137, v139\n"
      "v_mfma_f32_16x16x32_bf16 a[136:139], a[84:87], a[96:99], a[136:139]\n"
      "v_accvgpr_write_b32 a168, v136\n"
      "v_accvgpr_write_b32 a169, v137\n"
      "v_accvgpr_write_b32 a170, v138\n"
      "v_mfma_f32_16x16x32_bf16 a[140:143], a[84:87], a[100:103], a[140:143]\n"
      "v_accvgpr_write_b32 a171, v139\n"
      "v_cvt_pk_bf16_f32 v140, v140, v141\n"
      "v_cvt_pk_bf16_f32 v141, v142, v143\n"
      "v_mfma_f32_16x16x32_bf16 a[144:147], a[84:87], a[104:107], a[144:147]\n"
      "v_cvt_pk_bf16_f32 v142, v172, v173\n"
      "v_cvt_pk_bf16_f32 v143, v174, v175\n"
      "v_pk_max_i16 v140, v140, 0\n"
      "v_mfma_f32_16x16x32_bf16 a[148:151], a[84:87], a[108:111], a[148:151]\n"
      "v_pk_max_i16 v142, v142, 0\n"
      "v_pk_max_i16 v141, v141, 0\n"
      "v_pk_max_i16 v143, v143, 0\n"
      "v_mfma_f32_16x16x32_bf16 a[152:155], a[84:87], a[112:115], a[152:155]\n"
      "v_permlane16_swap_b32 v140, v142\n"
      "s_nop 0\n"
      "v_permlane16_swap_b32 v141, v143\n"
      "v_mfma_f32_16x16x32_bf16 a[156:159], a[84:87], a[116:119], a[156:159]\n"
      "v_accvgpr_write_b32 a172, v140\n"
      "v_accvgpr_write_b32 a173, v141\n"
      "v_accvgpr_write_b32 a174, v142\n"
      "v_accvgpr_write_b32 a175, v143\n"
      "v_cvt_pk_bf16_f32 v144, v144, v145\n"
      "v_cvt_pk_bf16_f32 v145, v146, v147\n"
      "v_cvt_pk_bf16_f32 v146, v176, v177\n"
      "v_cvt_pk_bf16_f32 v147, v178, v179\n"
      "v_pk_max_i16 v144, v144, 0\n"
      "v_pk_max_i16 v146, v146, 0\n"
      "v_pk_max_i16 v145, v145, 0\n"
      "v_pk_max_i16 v147, v147, 0\n"
      "v_permlane16_swap_b32 v144, v146\n"
      "s_nop 0\n"
      "v_permlane16_swap_b32 v145, v147\n"
      "v_accvgpr_write_b32 a176, v144\n"
      "v_accvgpr_write_b32 a177, v145\n"
      "v_accvgpr_write_b32 a178, v146\n"
      "v_accvgpr_write_b32 a179, v147\n"
      "v_cvt_pk_bf16_f32 v148, v148, v149\n"
      "v_cvt_pk_bf16_f32 v149, v150, v151\n"
      "v_cvt_pk_bf16_f32 v150, v180, v181\n"
      "v_cvt_pk_bf16_f32 v151, v182, v183\n"
      "v_pk_max_i16 v148, v148, 0\n"
      "v_pk_max_i16 v150, v150, 0\n"
      "v_pk_max_i16 v149, v149, 0\n"
      "v_pk_max_i16 v151, v151, 0\n"
      "v_permlane16_swap_b32 v148, v150\n"
      "s_nop 0\n"
      "v_permlane16_swap_b32 v149, v151\n"
      "v_accvgpr_write_b32 a180, v148\n"
      "v_accvgpr_write_b32 a181, v149\n"
      "v_accvgpr_write_b32 a182, v150\n"
      "v_accvgpr_write_b32 a183, v151\n"
      "v_cvt_pk_bf16_f32 v152, v152, v153\n"
      "v_cvt_pk_bf16_f32 v153, v154, v155\n"
      "v_cvt_pk_bf16_f32 v154, v184, v185\n"
      "v_cvt_pk_bf16_f32 v155, v186, v187\n"
      "v_pk_max_i16 v152, v152, 0\n"
      "v_pk_max_i16 v154, v154, 0\n"
      "v_pk_max_i16 v153, v153, 0\n"
      "v_pk_max_i16 v155, v155, 0\n"
      "v_permlane16_swap_b32 v152, v154\n"
      "s_nop 0\n"
      "v_permlane16_swap_b32 v153, v155\n"
      "v_accvgpr_write_b32 a184, v152\n"
      "v_accvgpr_write_b32 a185, v153\n"
      "v_accvgpr_write_b32 a186, v154\n"
      "v_accvgpr_write_b32 a187, v155\n"
      "v_cvt_pk_bf16_f32 v156, v156, v157\n"
      "v_cvt_pk_bf16_f32 v157, v158, v159\n"
      "v_cvt_pk_bf16_f32 v158, v188, v189\n"
      "v_cvt_pk_bf16_f32 v159, v190, v191\n"
      "v_pk_max_i16 v156, v156, 0\n"
      "v_pk_max_i16 v158, v158, 0\n"
      "v_pk_max_i16 v157, v157, 0\n"
      "v_pk_max_i16 v159, v159, 0\n"
      "v_permlane16_swap_b32 v156, v158\n"
      "s_nop 0\n"
      "v_permlane16_swap_b32 v157, v159\n"
      "v_accvgpr_write_b32 a188, v156\n"
      "v_accvgpr_write_b32 a189, v157\n"
      "v_accvgpr_write_b32 a190, v158\n"
      "v_accvgpr_write_b32 a191, v159\n"
      "s_waitcnt lgkmcnt(0)\n"
      "v_mfma_f32_16x16x32_bf16 v[64:67], a[0:3], a[16:19], 0\n"
      "ds_read_b128 a[48:51], v46 offset:32768\n"
      "s_mov_b32 s20, s48\n"
      "v_mfma_f32_16x16x32_bf16 v[68:71], a[0:3], a[20:23], 0\n"
      "ds_read_b128 a[64:67], v48 offset:0\n"
      "s_and_b32 s21, s49, 0xffff\n"
      "v_mfma_f32_16x16x32_bf16 v[72:75], a[0:3], a[24:27], 0\n"
      "s_mov_b32 s22, 0x700000\n"
      "s_mov_b32 s23, 0x20000\n"
      "v_mfma_f32_16x16x32_bf16 v[76:79], a[0:3], a[28:31], 0\n"
      "ds_read_b128 a[68:71], v48 offset:2048\n"
      "v_accvgpr_read_b32 v23, a221\n"
      "v_mfma_f32_16x16x32_bf16 v[80:83], a[0:3], a[32:35], 0\n"
      "v_accvgpr_read_b32 v24, a222\n"
      "buffer_store_dwordx4 a[160:163], v8, s[64:67], s73 offen offset:0 sc1\n"
      "v_mfma_f32_16x16x32_bf16 v[84:87], a[0:3], a[36:39], 0\n"
      "ds_read_b128 a[72:75], v48 offset:4096\n"
      "v_cvt_pk_bf16_f32 v192, v192, v193\n"
      "v_mfma_f32_16x16x32_bf16 v[88:91], a[0:3], a[40:43], 0\n"
      "ds_read_b128 a[76:79], v48 offset:6144\n"
      "v_cvt_pk_bf16_f32 v193, v194, v195\n"
      "v_mfma_f32_16x16x32_bf16 v[92:95], a[0:3], a[44:47], 0\n"
      "v_cvt_pk_bf16_f32 v194, v224, v225\n"
      "v_cvt_pk_bf16_f32 v195, v226, v227\n"
      "v_mfma_f32_16x16x32_bf16 v[124:127], a[4:7], a[44:47], 0\n"
      "ds_read_b128 a[80:83], v48 offset:8192\n"
      "v_pk_max_i16 v192, v192, 0\n"
      "v_mfma_f32_16x16x32_bf16 v[120:123], a[4:7], a[40:43], 0\n"
      "v_pk_max_i16 v194, v194, 0\n"
      "v_pk_max_i16 v193, v193, 0\n"
      "v_mfma_f32_16x16x32_bf16 v[116:119], a[4:7], a[36:39], 0\n"
      "ds_read_b128 a[84:87], v48 offset:10240\n"
      "v_pk_max_i16 v195, v195, 0\n"
      "v_mfma_f32_16x16x32_bf16 v[112:115], a[4:7], a[32:35], 0\n"
      "ds_read_b128 a[88:91], v48 offset:12288\n"
      "v_permlane16_swap_b32 v192, v194\n"
      "v_mfma_f32_16x16x32_bf16 v[108:111], a[4:7], a[28:31], 0\n"
      "s_nop 0\n"
      "v_permlane16_swap_b32 v193, v195\n"
      "v_mfma_f32_16x16x32_bf16 v[104:107], a[4:7], a[24:27], 0\n"
      "ds_read_b128 a[92:95], v48 offset:14336\n"
      "buffer_store_dwordx4 v[192:195], v8, s[64:67], s74 offen offset:0 sc1\n"
      "v_mfma_f32_16x16x32_bf16 v[100:103], a[4:7], a[20:23], 0\n"
      "v_mfma_f32_16x16x32_bf16 v[96:99], a[4:7], a[16:19], 0\n"
      "ds_read_b128 a[52:55], v46 offset:34816\n"
      "v_mfma_f32_16x16x32_bf16 v[128:131], a[8:11], a[16:19], 0\n"
      "ds_read_b128 a[56:59], v46 offset:36864\n"
      "v_mfma_f32_16x16x32_bf16 v[132:135], a[8:11], a[20:23], 0\n"
      "v_mfma_f32_16x16x32_bf16 v[136:139], a[8:11], a[24:27], 0\n"
      "ds_read_b128 a[60:63], v46 offset:38912\n"
      "v_mfma_f32_16x16x32_bf16 v[140:143], a[8:11], a[28:31], 0\n"
      "v_mfma_f32_16x16x32_bf16 v[144:147], a[8:11], a[32:35], 0\n"
      "buffer_store_dwordx4 a[164:167], v8, s[64:67], s73 offen offset:2048 sc1\n"
      "v_cvt_pk_bf16_f32 v196, v196, v197\n"
      "v_mfma_f32_16x16x32_bf16 v[148:151], a[8:11], a[36:39], 0\n"
      "v_cvt_pk_bf16_f32 v197, v198, v199\n"
      "v_cvt_pk_bf16_f32 v198, v228, v229\n"
      "v_mfma_f32_16x16x32_bf16 v[152:155], a[8:11], a[40:43], 0\n"
      "v_cvt_pk_bf16_f32 v199, v230, v231\n"
      "v_pk_max_i16 v196, v196, 0\n"
      "v_mfma_f32_16x16x32_bf16 v[156:159], a[8:11], a[44:47], 0\n"
      "v_pk_max_i16 v198, v198, 0\n"
      "v_pk_max_i16 v197, v197, 0\n"
      "v_mfma_f32_16x16x32_bf16 v[188:191], a[12:15], a[44:47], 0\n"
      "v_pk_max_i16 v199, v199, 0\n"
      "v_permlane16_swap_b32 v196, v198\n"
      "v_mfma_f32_16x16x32_bf16 v[184:187], a[12:15], a[40:43], 0\n"
      "s_add_u32 s96, s94, s85\n"
      "s_add_u32 s95, s94, s92\n"
      "v_mfma_f32_16x16x32_bf16 v[180:183], a[12:15], a[36:39], 0\n"
      "s_add_u32 s95, s95, 32768\n"
      "s_cmp_eq_u32 s94, 98304\n"
      "v_mfma_f32_16x16x32_bf16 v[176:179], a[12:15], a[32:35], 0\n"
      "s_cselect_b32 s97, 0xfffe8000, s99\n"
      "s_add_u32 s94, s94, s97\n"
      "v_mfma_f32_16x16x32_bf16 v[172:175], a[12:15], a[28:31], 0\n"
      "v_add_u32 v45, s97, v45\n"
      "v_add_u32 v46, s97, v46\n"
      "v_mfma_f32_16x16x32_bf16 v[168:171], a[12:15], a[24:27], 0\n"
      "v_add_u32 v47, s97, v47\n"
      "v_add_u32 v48, s97, v48\n"
      "v_mfma_f32_16x16x32_bf16 v[164:167], a[12:15], a[20:23], 0\n"
      "s_nop 0\n"
      "v_permlane16_swap_b32 v197, v199\n"
      "v_mfma_f32_16x16x32_bf16 v[160:163], a[12:15], a[16:19], 0\n"
      "buffer_store_dwordx4 v[196:199], v8, s[64:67], s74 offen offset:2048 sc1\n"
      "s_waitcnt lgkmcnt(0)\n"
      "v_mfma_f32_16x16x32_bf16 v[64:67], a[48:51], a[64:67], v[64:67]\n"
      "s_waitcnt vmcnt(24)\n"
      "s_barrier\n"
      "v_mfma_f32_16x16x32_bf16 v[68:71], a[48:51], a[68:71], v[68:71]\n"
      "ds_read_b128 a[0:3], v45 offset:32768\n"
      "v_mfma_f32_16x16x32_bf16 v[72:75], a[48:51], a[72:75], v[72:75]\n"
      "ds_read_b128 a[16:19], v47 offset:0\n"
      "s_mov_b32 m0, s95\n"
      "v_mfma_f32_16x16x32_bf16 v[76:79], a[48:51], a[76:79], v[76:79]\n"
      "buffer_load_dwordx4 v33, s[64:67], s28 offen offset:0 sc1 lds\n"
      "v_mfma_f32_16x16x32_bf16 v[80:83], a[48:51], a[80:83], v[80:83]\n"
      "ds_read_b128 a[20:23], v47 offset:2048\n"
      "buffer_store_dwordx4 a[168:171], v9, s[64:67], s73 offen offset:0 sc1\n"
      "v_mfma_f32_16x16x32_bf16 v[84:87], a[48:51], a[84:87], v[84:87]\n"
      "ds_read_b128 a[24:27], v47 offset:4096\n"
      "v_cvt_pk_bf16_f32 v200, v200, v201\n"
      "v_mfma_f32_16x16x32_bf16 v[88:91], a[48:51], a[88:91], v[88:91]\n"
      "ds_read_b128 a[28:31], v47 offset:6144\n"
      "v_cvt_pk_bf16_f32 v201, v202, v203\n"
      "v_mfma_f32_16x16x32_bf16 v[92:95], a[48:51], a[92:95], v[92:95]\n"
      "ds_read_b128 a[32:35], v47 offset:8192\n"
      "v_cvt_pk_bf16_f32 v202, v232, v233\n"
      "v_mfma_f32_16x16x32_bf16 v[124:127], a[52:55], a[92:95], v[124:127]\n"
      "buffer_load_dwordx4 v34, s[64:67], s28 offen offset:1024 sc1 lds\n"
      "v_cvt_pk_bf16_f32 v203, v234, v235\n"
      "v_mfma_f32_16x16x32_bf16 v[120:123], a[52:55], a[88:91], v[120:123]\n"
      "ds_read_b128 a[36:39], v47 offset:10240\n"
      "v_pk_max_i16 v200, v200, 0\n"
      "v_mfma_f32_16x16x32_bf16 v[116:119], a[52:55], a[84:87], v[116:119]\n"
      "ds_read_b128 a[40:43], v47 offset:12288\n"
      "v_pk_max_i16 v202, v202, 0\n"
      "v_mfma_f32_16x16x32_bf16 v[112:115], a[52:55], a[80:83], v[112:115]\n"
      "ds_read_b128 a[44:47], v47 offset:14336\n"
      "v_pk_max_i16 v201, v201, 0\n"
      "v_mfma_f32_16x16x32_bf16 v[108:111], a[52:55], a[76:79], v[108:111]\n"
      "ds_read_b128 a[4:7], v45 offset:34816\n"
      "v_pk_max_i16 v203, v203, 0\n"
      "v_mfma_f32_16x16x32_bf16 v[104:107], a[52:55], a[72:75], v[104:107]\n"
      "buffer_load_dwordx4 v35, s[64:67], s28 offen offset:2048 sc1 lds\n"
      "v_permlane16_swap_b32 v200, v202\n"
      "v_mfma_f32_16x16x32_bf16 v[100:103], a[52:55], a[68:71], v[100:103]\n"
      "ds_read_b128 a[8:11], v45 offset:36864\n"
      "s_nop 0\n"
      "v_mfma_f32_16x16x32_bf16 v[96:99], a[52:55], a[64:67], v[96:99]\n"
      "ds_read_b128 a[12:15], v45 offset:38912\n"
      "v_permlane16_swap_b32 v201, v203\n"
      "v_mfma_f32_16x16x32_bf16 v[128:131], a[56:59], a[64:67], v[128:131]\n"
      "buffer_store_dwordx4 v[200:203], v9, s[64:67], s74 offen offset:0 sc1\n"
      "v_mfma_f32_16x16x32_bf16 v[132:135], a[56:59], a[68:71], v[132:135]\n"
      "v_mfma_f32_16x16x32_bf16 v[136:139], a[56:59], a[72:75], v[136:139]\n"
      "buffer_load_dwordx4 v36, s[64:67], s28 offen offset:3072 sc1 lds\n"
      "v_mfma_f32_16x16x32_bf16 v[140:143], a[56:59], a[76:79], v[140:143]\n"
      "s_and_b32 s83, s10, 15\n"
      "v_mfma_f32_16x16x32_bf16 v[144:147], a[56:59], a[80:83], v[144:147]\n"
      "s_lshr_b64 s[10:11], s[10:11], 4\n"
      "v_mfma_f32_16x16x32_bf16 v[148:151], a[56:59], a[84:87], v[148:151]\n"
      "s_mul_i32 s28, s83, s30\n"
      "v_mfma_f32_16x16x32_bf16 v[152:155], a[56:59], a[88:91], v[152:155]\n"
      "s_add_u32 s28, s28, s81\n"
      "s_add_u32 m0, s96, 0\n"
      "v_mfma_f32_16x16x32_bf16 v[156:159], a[56:59], a[92:95], v[156:159]\n"
      "buffer_load_dwordx4 v37, s[24:27], s29 offen offset:0 lds\n"
      "v_mfma_f32_16x16x32_bf16 v[188:191], a[60:63], a[92:95], v[188:191]\n"
      "buffer_store_dwordx4 a[172:175], v9, s[64:67], s73 offen offset:2048 sc1\n"
      "v_cvt_pk_bf16_f32 v204, v204, v205\n"
      "v_mfma_f32_16x16x32_bf16 v[184:187], a[60:63], a[88:91], v[184:187]\n"
      "v_cvt_pk_bf16_f32 v205, v206, v207\n"
      "v_cvt_pk_bf16_f32 v206, v236, v237\n"
      "v_mfma_f32_16x16x32_bf16 v[180:183], a[60:63], a[84:87], v[180:183]\n"
      "v_cvt_pk_bf16_f32 v207, v238, v239\n"
      "v_pk_max_i16 v204, v204, 0\n"
      "v_mfma_f32_16x16x32_bf16 v[176:179], a[60:63], a[80:83], v[176:179]\n"
      "v_pk_max_i16 v206, v206, 0\n"
      "v_pk_max_i16 v205, v205, 0\n"
      "v_mfma_f32_16x16x32_bf16 v[172:175], a[60:63], a[76:79], v[172:175]\n"
      "buffer_load_dwordx4 v38, s[24:27], s29 offen offset:1024 lds\n"
      "v_pk_max_i16 v207, v207, 0\n"
      "v_mfma_f32_16x16x32_bf16 v[168:171], a[60:63], a[72:75], v[168:171]\n"
      "v_permlane16_swap_b32 v204, v206\n"
      "s_nop 0\n"
      "v_mfma_f32_16x16x32_bf16 v[164:167], a[60:63], a[68:71], v[164:167]\n"
      "v_permlane16_swap_b32 v205, v207\n"
      "buffer_store_dwordx4 v[204:207], v9, s[64:67], s74 offen offset:2048 sc1\n"
      "v_mfma_f32_16x16x32_bf16 v[160:163], a[60:63], a[64:67], v[160:163]\n"
      "s_waitcnt lgkmcnt(0)\n"
      "v_mfma_f32_16x16x32_bf16 v[64:67], a[0:3], a[16:19], v[64:67]\n"
      "ds_read_b128 a[48:51], v46 offset:32768\n"
      "v_mfma_f32_16x16x32_bf16 v[68:71], a[0:3], a[20:23], v[68:71]\n"
      "ds_read_b128 a[64:67], v48 offset:0\n"
      "v_mfma_f32_16x16x32_bf16 v[72:75], a[0:3], a[24:27], v[72:75]\n"
      "ds_read_b128 a[68:71], v48 offset:2048\n"
      "v_mfma_f32_16x16x32_bf16 v[76:79], a[0:3], a[28:31], v[76:79]\n"
      "buffer_load_dwordx4 v39, s[24:27], s29 offen offset:2048 lds\n"
      "v_mfma_f32_16x16x32_bf16 v[80:83], a[0:3], a[32:35], v[80:83]\n"
      "ds_read_b128 a[72:75], v48 offset:4096\n"
      "buffer_store_dwordx4 a[176:179], v10, s[64:67], s73 offen offset:0 sc1\n"
      "v_mfma_f32_16x16x32_bf16 v[84:87], a[0:3], a[36:39], v[84:87]\n"
      "ds_read_b128 a[76:79], v48 offset:6144\n"
      "v_cvt_pk_bf16_f32 v208, v208, v209\n"
      "v_mfma_f32_16x16x32_bf16 v[88:91], a[0:3], a[40:43], v[88:91]\n"
      "ds_read_b128 a[80:83], v48 offset:8192\n"
      "v_cvt_pk_bf16_f32 v209, v210, v211\n"
      "v_mfma_f32_16x16x32_bf16 v[92:95], a[0:3], a[44:47], v[92:95]\n"
      "ds_read_b128 a[84:87], v48 offset:10240\n"
      "v_cvt_pk_bf16_f32 v210, v240, v241\n"
      "v_mfma_f32_16x16x32_bf16 v[124:127], a[4:7], a[44:47], v[124:127]\n"
      "buffer_load_dwordx4 v40, s[24:27], s29 offen offset:3072 lds\n"
      "v_cvt_pk_bf16_f32 v211, v242, v243\n"
      "v_mfma_f32_16x16x32_bf16 v[120:123], a[4:7], a[40:43], v[120:123]\n"
      "ds_read_b128 a[88:91], v48 offset:12288\n"
      "v_pk_max_i16 v208, v208, 0\n"
      "v_mfma_f32_16x16x32_bf16 v[116:119], a[4:7], a[36:39], v[116:119]\n"
      "ds_read_b128 a[92:95], v48 offset:14336\n"
      "v_pk_max_i16 v210, v210, 0\n"
      "v_mfma_f32_16x16x32_bf16 v[112:115], a[4:7], a[32:35], v[112:115]\n"
      "ds_read_b128 a[52:55], v46 offset:34816\n"
      "v_pk_max_i16 v209, v209, 0\n"
      "v_mfma_f32_16x16x32_bf16 v[108:111], a[4:7], a[28:31], v[108:111]\n"
      "ds_read_b128 a[56:59], v46 offset:36864\n"
      "s_add_u32 m0, s96, 4096\n"
      "v_mfma_f32_16x16x32_bf16 v[104:107], a[4:7], a[24:27], v[104:107]\n"
      "buffer_load_dwordx4 v41, s[24:27], s29 offen offset:0 lds\n"
      "v_pk_max_i16 v211, v211, 0\n"
      "v_mfma_f32_16x16x32_bf16 v[100:103], a[4:7], a[20:23], v[100:103]\n"
      "ds_read_b128 a[60:63], v46 offset:38912\n"
      "v_permlane16_swap_b32 v208, v210\n"
      "v_mfma_f32_16x16x32_bf16 v[96:99], a[4:7], a[16:19], v[96:99]\n"
      "s_add_u32 s96, s94, s85\n"
      "s_nop 0\n"
      "v_mfma_f32_16x16x32_bf16 v[128:131], a[8:11], a[16:19], v[128:131]\n"
      "s_add_u32 s95, s94, s92\n"
      "s_add_u32 s95, s95, 32768\n"
      "v_mfma_f32_16x16x32_bf16 v[132:135], a[8:11], a[20:23], v[132:135]\n"
      "s_cmp_eq_u32 s94, 98304\n"
      "s_cselect_b32 s97, 0xfffe8000, s99\n"
      "s_add_u32 s94, s94, s97\n"
      "v_mfma_f32_16x16x32_bf16 v[136:139], a[8:11], a[24:27], v[136:139]\n"
      "buffer_load_dwordx4 v42, s[24:27], s29 offen offset:1024 lds\n"
      "v_permlane16_swap_b32 v209, v211\n"
      "v_mfma_f32_16x16x32_bf16 v[140:143], a[8:11], a[28:31], v[140:143]\n"
      "v_add_u32 v45, s97, v45\n"
      "v_add_u32 v46, s97, v46\n"
      "v_mfma_f32_16x16x32_bf16 v[144:147], a[8:11], a[32:35], v[144:147]\n"
      "v_add_u32 v47, s97, v47\n"
      "v_add_u32 v48, s97, v48\n"
      "v_mfma_f32_16x16x32_bf16 v[148:151], a[8:11], a[36:39], v[148:151]\n"
      "buffer_store_dwordx4 v[208:211], v10, s[64:67], s74 offen offset:0 sc1\n"
      "v_mfma_f32_16x16x32_bf16 v[152:155], a[8:11], a[40:43], v[152:155]\n"
      "v_mfma_f32_16x16x32_bf16 v[156:159], a[8:11], a[44:47], v[156:159]\n"
      "buffer_load_dwordx4 v43, s[24:27], s29 offen offset:2048 lds\n"
      "v_mfma_f32_16x16x32_bf16 v[188:191], a[12:15], a[44:47], v[188:191]\n"
      "v_mfma_f32_16x16x32_bf16 v[184:187], a[12:15], a[40:43], v[184:187]\n"
      "v_mfma_f32_16x16x32_bf16 v[180:183], a[12:15], a[36:39], v[180:183]\n"
      "v_mfma_f32_16x16x32_bf16 v[176:179], a[12:15], a[32:35], v[176:179]\n"
      "buffer_store_dwordx4 a[180:183], v10, s[64:67], s73 offen offset:2048 sc1\n"
      "v_cvt_pk_bf16_f32 v212, v212, v213\n"
      "v_mfma_f32_16x16x32_bf16 v[172:175], a[12:15], a[28:31], v[172:175]\n"
      "buffer_load_dwordx4 v44, s[24:27], s29 offen offset:3072 lds\n"
      "v_cvt_pk_bf16_f32 v213, v214, v215\n"
      "v_mfma_f32_16x16x32_bf16 v[168:171], a[12:15], a[24:27], v[168:171]\n"
      "s_lshl_b32 s29, s83, 7\n"
      "s_add_u32 s29, s29, s82\n"
      "v_mfma_f32_16x16x32_bf16 v[164:167], a[12:15], a[20:23], v[164:167]\n"
      "v_cvt_pk_bf16_f32 v214, v244, v245\n"
      "v_cvt_pk_bf16_f32 v215, v246, v247\n"
      "v_mfma_f32_16x16x32_bf16 v[160:163], a[12:15], a[16:19], v[160:163]\n"
      "v_pk_max_i16 v212, v212, 0\n"
      "v_pk_max_i16 v214, v214, 0\n"
      "s_waitcnt lgkmcnt(0)\n"
      "v_mfma_f32_16x16x32_bf16 v[64:67], a[48:51], a[64:67], v[64:67]\n"
      "s_waitcnt vmcnt(25)\n"
      "s_barrier\n"
      "v_mfma_f32_16x16x32_bf16 v[68:71], a[48:51], a[68:71], v[68:71]\n"
      "ds_read_b128 a[0:3], v45 offset:32768\n"
      "v_pk_max_i16 v213, v213, 0\n"
      "v_mfma_f32_16x16x32_bf16 v[72:75], a[48:51], a[72:75], v[72:75]\n"
      "ds_read_b128 a[16:19], v47 offset:0\n"
      "s_mov_b32 m0, s95\n"
      "v_mfma_f32_16x16x32_bf16 v[76:79], a[48:51], a[76:79], v[76:79]\n"
      "buffer_load_dwordx4 v33, s[64:67], s28 offen offset:0 sc1 lds\n"
      "v_pk_max_i16 v215, v215, 0\n"
      "v_mfma_f32_16x16x32_bf16 v[80:83], a[48:51], a[80:83], v[80:83]\n"
      "ds_read_b128 a[20:23], v47 offset:2048\n"
      "v_permlane16_swap_b32 v212, v214\n"
      "v_mfma_f32_16x16x32_bf16 v[84:87], a[48:51], a[84:87], v[84:87]\n"
      "ds_read_b128 a[24:27], v47 offset:4096\n"
      "s_nop 0\n"
      "v_mfma_f32_16x16x32_bf16 v[88:91], a[48:51], a[88:91], v[88:91]\n"
      "ds_read_b128 a[28:31], v47 offset:6144\n"
      "v_permlane16_swap_b32 v213, v215\n"
      "v_mfma_f32_16x16x32_bf16 v[92:95], a[48:51], a[92:95], v[92:95]\n"
      "ds_read_b128 a[32:35], v47 offset:8192\n"
      "buffer_store_dwordx4 v[212:215], v10, s[64:67], s74 offen offset:2048 sc1\n"
      "v_mfma_f32_16x16x32_bf16 v[124:127], a[52:55], a[92:95], v[124:127]\n"
      "buffer_load_dwordx4 v34, s[64:67], s28 offen offset:1024 sc1 lds\n"
      "v_mfma_f32_16x16x32_bf16 v[120:123], a[52:55], a[88:91], v[120:123]\n"
      "ds_read_b128 a[36:39], v47 offset:10240\n"
      "v_mfma_f32_16x16x32_bf16 v[116:119], a[52:55], a[84:87], v[116:119]\n"
      "ds_read_b128 a[40:43], v47 offset:12288\n"
      "v_mfma_f32_16x16x32_bf16 v[112:115], a[52:55], a[80:83], v[112:115]\n"
      "ds_read_b128 a[44:47], v47 offset:14336\n"
      "v_mfma_f32_16x16x32_bf16 v[108:111], a[52:55], a[76:79], v[108:111]\n"
      "ds_read_b128 a[4:7], v45 offset:34816\n"
      "v_mfma_f32_16x16x32_bf16 v[104:107], a[52:55], a[72:75], v[104:107]\n"
      "buffer_load_dwordx4 v35, s[64:67], s28 offen offset:2048 sc1 lds\n"
      "v_mfma_f32_16x16x32_bf16 v[100:103], a[52:55], a[68:71], v[100:103]\n"
      "ds_read_b128 a[8:11], v45 offset:36864\n"
      "buffer_store_dwordx4 a[184:187], v11, s[64:67], s73 offen offset:0 sc1\n"
      "v_mfma_f32_16x16x32_bf16 v[96:99], a[52:55], a[64:67], v[96:99]\n"
      "ds_read_b128 a[12:15], v45 offset:38912\n"
      "v_cvt_pk_bf16_f32 v216, v216, v217\n"
      "v_mfma_f32_16x16x32_bf16 v[128:131], a[56:59], a[64:67], v[128:131]\n"
      "v_cvt_pk_bf16_f32 v217, v218, v219\n"
      "v_cvt_pk_bf16_f32 v218, v248, v249\n"
      "v_mfma_f32_16x16x32_bf16 v[132:135], a[56:59], a[68:71], v[132:135]\n"
      "v_cvt_pk_bf16_f32 v219, v250, v251\n"
      "v_pk_max_i16 v216, v216, 0\n"
      "v_mfma_f32_16x16x32_bf16 v[136:139], a[56:59], a[72:75], v[136:139]\n"
      "buffer_load_dwordx4 v36, s[64:67], s28 offen offset:3072 sc1 lds\n"
      "v_pk_max_i16 v218, v218, 0\n"
      "v_mfma_f32_16x16x32_bf16 v[140:143], a[56:59], a[76:79], v[140:143]\n"
      "s_and_b32 s83, s10, 15\n"
      "v_pk_max_i16 v217, v217, 0\n"
      "v_mfma_f32_16x16x32_bf16 v[144:147], a[56:59], a[80:83], v[144:147]\n"
      "s_lshr_b64 s[10:11], s[10:11], 4\n"
      "v_pk_max_i16 v219, v219, 0\n"
      "v_mfma_f32_16x16x32_bf16 v[148:151], a[56:59], a[84:87], v[148:151]\n"
      "s_mul_i32 s28, s83, s30\n"
      "v_permlane16_swap_b32 v216, v218\n"
      "v_mfma_f32_16x16x32_bf16 v[152:155], a[56:59], a[88:91], v[152:155]\n"
      "s_add_u32 s28, s28, s81\n"
      "s_add_u32 m0, s96, 0\n"
      "v_mfma_f32_16x16x32_bf16 v[156:159], a[56:59], a[92:95], v[156:159]\n"
      "buffer_load_dwordx4 v37, s[24:27], s29 offen offset:0 lds\n"
      "s_nop 0\n"
      "v_mfma_f32_16x16x32_bf16 v[188:191], a[60:63], a[92:95], v[188:191]\n"
      "v_permlane16_swap_b32 v217, v219\n"
      "buffer_store_dwordx4 v[216:219], v11, s[64:67], s74 offen offset:0 sc1\n"
      "v_mfma_f32_16x16x32_bf16 v[184:187], a[60:63], a[88:91], v[184:187]\n"
      "v_mfma_f32_16x16x32_bf16 v[180:183], a[60:63], a[84:87], v[180:183]\n"
      "v_mfma_f32_16x16x32_bf16 v[176:179], a[60:63], a[80:83], v[176:179]\n"
      "v_mfma_f32_16x16x32_bf16 v[172:175], a[60:63], a[76:79], v[172:175]\n"
      "buffer_load_dwordx4 v38, s[24:27], s29 offen offset:1024 lds\n"
      "v_mfma_f32_16x16x32_bf16 v[168:171], a[60:63], a[72:75], v[168:171]\n"
      "v_mfma_f32_16x16x32_bf16 v[164:167], a[60:63], a[68:71], v[164:167]\n"
      "buffer_store_dwordx4 a[188:191], v11, s[64:67], s73 offen offset:2048 sc1\n"
      "v_cvt_pk_bf16_f32 v220, v220, v221\n"
      "v_mfma_f32_16x16x32_bf16 v[160:163], a[60:63], a[64:67], v[160:163]\n"
      "v_cvt_pk_bf16_f32 v221, v222, v223\n"
      "v_cvt_pk_bf16_f32 v222, v252, v253\n"
      "s_waitcnt lgkmcnt(0)\n"
      "v_mfma_f32_16x16x32_bf16 v[64:67], a[0:3], a[16:19], v[64:67]\n"
      "ds_read_b128 a[48:51], v46 offset:32768\n"
      "v_cvt_pk_bf16_f32 v223, v254, v255\n"
      "v_mfma_f32_16x16x32_bf16 v[68:71], a[0:3], a[20:23], v[68:71]\n"
      "ds_read_b128 a[64:67], v48 offset:0\n"
      "v_pk_max_i16 v220, v220, 0\n"
      "v_mfma_f32_16x16x32_bf16 v[72:75], a[0:3], a[24:27], v[72:75]\n"
      "ds_read_b128 a[68:71], v48 offset:2048\n"
      "v_pk_max_i16 v222, v222, 0\n"
      "v_mfma_f32_16x16x32_bf16 v[76:79], a[0:3], a[28:31], v[76:79]\n"
      "buffer_load_dwordx4 v39, s[24:27], s29 offen offset:2048 lds\n"
      "v_pk_max_i16 v221, v221, 0\n"
      "v_mfma_f32_16x16x32_bf16 v[80:83], a[0:3], a[32:35], v[80:83]\n"
      "ds_read_b128 a[72:75], v48 offset:4096\n"
      "v_pk_max_i16 v223, v223, 0\n"
      "v_mfma_f32_16x16x32_bf16 v[84:87], a[0:3], a[36:39], v[84:87]\n"
      "ds_read_b128 a[76:79], v48 offset:6144\n"
      "v_permlane16_swap_b32 v220, v222\n"
      "v_mfma_f32_16x16x32_bf16 v[88:91], a[0:3], a[40:43], v[88:91]\n"
      "ds_read_b128 a[80:83], v48 offset:8192\n"
      "s_nop 0\n"
      "v_mfma_f32_16x16x32_bf16 v[92:95], a[0:3], a[44:47], v[92:95]\n"
      "ds_read_b128 a[84:87], v48 offset:10240\n"
      "v_permlane16_swap_b32 v221, v223\n"
      "v_mfma_f32_16x16x32_bf16 v[124:127], a[4:7], a[44:47], v[124:127]\n"
      "buffer_load_dwordx4 v40, s[24:27], s29 offen offset:3072 lds\n"
      "v_mfma_f32_16x16x32_bf16 v[120:123], a[4:7], a[40:43], v[120:123]\n"
      "ds_read_b128 a[88:91], v48 offset:12288\n"
      "buffer_store_dwordx4 v[220:223], v11, s[64:67], s74 offen offset:2048 sc1\n"
      "v_mfma_f32_16x16x32_bf16 v[116:119], a[4:7], a[36:39], v[116:119]\n"
      "ds_read_b128 a[92:95], v48 offset:14336\n"
      "v_accvgpr_read_b32 v224, a128\n"
      "v_mfma_f32_16x16x32_bf16 v[112:115], a[4:7], a[32:35], v[112:115]\n"
      "ds_read_b128 a[52:55], v46 offset:34816\n"
      "v_accvgpr_read_b32 v225, a129\n"
      "v_mfma_f32_16x16x32_bf16 v[108:111], a[4:7], a[28:31], v[108:111]\n"
      "ds_read_b128 a[56:59], v46 offset:36864\n"
      "s_add_u32 m0, s96, 4096\n"
      "v_mfma_f32_16x16x32_bf16 v[104:107], a[4:7], a[24:27], v[104:107]\n"
      "buffer_load_dwordx4 v41, s[24:27], s29 offen offset:0 lds\n"
      "v_accvgpr_read_b32 v226, a130\n"
      "v_mfma_f32_16x16x32_bf16 v[100:103], a[4:7], a[20:23], v[100:103]\n"
      "ds_read_b128 a[60:63], v46 offset:38912\n"
      "v_accvgpr_read_b32 v227, a131\n"
      "v_mfma_f32_16x16x32_bf16 v[96:99], a[4:7], a[16:19], v[96:99]\n"
      "s_add_u32 s96, s94, s85\n"
      "v_accvgpr_read_b32 v228, a132\n"
      "v_mfma_f32_16x16x32_bf16 v[128:131], a[8:11], a[16:19], v[128:131]\n"
      "s_add_u32 s95, s94, s92\n"
      "s_add_u32 s95, s95, 32768\n"
      "v_mfma_f32_16x16x32_bf16 v[132:135], a[8:11], a[20:23], v[132:135]\n"
      "s_cmp_eq_u32 s94, 98304\n"
      "s_cselect_b32 s97, 0xfffe8000, s99\n"
      "s_add_u32 s94, s94, s97\n"
      "v_mfma_f32_16x16x32_bf16 v[136:139], a[8:11], a[24:27], v[136:139]\n"
      "buffer_load_dwordx4 v42, s[24:27], s29 offen offset:1024 lds\n"
      "v_accvgpr_read_b32 v229, a133\n"
      "v_mfma_f32_16x16x32_bf16 v[140:143], a[8:11], a[28:31], v[140:143]\n"
      "v_add_u32 v45, s97, v45\n"
      "v_add_u32 v46, s97, v46\n"
      "v_mfma_f32_16x16x32_bf16 v[144:147], a[8:11], a[32:35], v[144:147]\n"
      "v_add_u32 v47, s97, v47\n"
      "v_add_u32 v48, s97, v48\n"
      "v_mfma_f32_16x16x32_bf16 v[148:151], a[8:11], a[36:39], v[148:151]\n"
      "v_accvgpr_read_b32 v230, a134\n"
      "v_accvgpr_read_b32 v231, a135\n"
      "v_mfma_f32_16x16x32_bf16 v[152:155], a[8:11], a[40:43], v[152:155]\n"
      "v_cvt_pk_bf16_f32 v224, v224, v225\n"
      "v_cvt_pk_bf16_f32 v225, v226, v227\n"
      "v_mfma_f32_16x16x32_bf16 v[156:159], a[8:11], a[44:47], v[156:159]\n"
      "buffer_load_dwordx4 v43, s[24:27], s29 offen offset:2048 lds\n"
      "v_cvt_pk_bf16_f32 v226, v228, v229\n"
      "v_mfma_f32_16x16x32_bf16 v[188:191], a[12:15], a[44:47], v[188:191]\n"
      "v_cvt_pk_bf16_f32 v227, v230, v231\n"
      "v_pk_max_i16 v224, v224, 0\n"
      "v_mfma_f32_16x16x32_bf16 v[184:187], a[12:15], a[40:43], v[184:187]\n"
      "v_pk_max_i16 v226, v226, 0\n"
      "v_pk_max_i16 v225, v225, 0\n"
      "v_mfma_f32_16x16x32_bf16 v[180:183], a[12:15], a[36:39], v[180:183]\n"
      "v_pk_max_i16 v227, v227, 0\n"
      "v_permlane16_swap_b32 v224, v226\n"
      "v_mfma_f32_16x16x32_bf16 v[176:179], a[12:15], a[32:35], v[176:179]\n"
      "s_nop 0\n"
      "v_permlane16_swap_b32 v225, v227\n"
      "v_mfma_f32_16x16x32_bf16 v[172:175], a[12:15], a[28:31], v[172:175]\n"
      "buffer_load_dwordx4 v44, s[24:27], s29 offen offset:3072 lds\n"
      "v_mfma_f32_16x16x32_bf16 v[168:171], a[12:15], a[24:27], v[168:171]\n"
      "s_lshl_b32 s29, s83, 7\n"
      "s_add_u32 s29, s29, s82\n"
      "v_mfma_f32_16x16x32_bf16 v[164:167], a[12:15], a[20:23], v[164:167]\n"
      "buffer_store_dwordx4 v[224:227], v12, s[64:67], s75 offen sc1\n"
      "v_accvgpr_read_b32 v232, a136\n"
      "v_mfma_f32_16x16x32_bf16 v[160:163], a[12:15], a[16:19], v[160:163]\n"
      "v_accvgpr_read_b32 v233, a137\n"
      "v_accvgpr_read_b32 v234, a138\n"
      "s_waitcnt lgkmcnt(0)\n"
      "v_mfma_f32_16x16x32_bf16 v[64:67], a[48:51], a[64:67], v[64:67]\n"
      "s_waitcnt vmcnt(18)\n"
      "s_barrier\n"
      "v_mfma_f32_16x16x32_bf16 v[68:71], a[48:51], a[68:71], v[68:71]\n"
      "ds_read_b128 a[0:3], v45 offset:32768\n"
      "v_accvgpr_read_b32 v235, a139\n"
      "v_mfma_f32_16x16x32_bf16 v[72:75], a[48:51], a[72:75], v[72:75]\n"
      "ds_read_b128 a[16:19], v47 offset:0\n"
      "s_mov_b32 m0, s95\n"
      "v_mfma_f32_16x16x32_bf16 v[76:79], a[48:51], a[76:79], v[76:79]\n"
      "buffer_load_dwordx4 v33, s[64:67], s28 offen offset:0 sc1 lds\n"
      "v_accvgpr_read_b32 v236, a140\n"
      "v_mfma_f32_16x16x32_bf16 v[80:83], a[48:51], a[80:83], v[80:83]\n"
      "ds_read_b128 a[20:23], v47 offset:2048\n"
      "v_accvgpr_read_b32 v237, a141\n"
      "v_mfma_f32_16x16x32_bf16 v[84:87], a[48:51], a[84:87], v[84:87]\n"
      "ds_read_b128 a[24:27], v47 offset:4096\n"
      "v_accvgpr_read_b32 v238, a142\n"
      "v_mfma_f32_16x16x32_bf16 v[88:91], a[48:51], a[88:91], v[88:91]\n"
      "ds_read_b128 a[28:31], v47 offset:6144\n"
      "v_accvgpr_read_b32 v239, a143\n"
      "v_mfma_f32_16x16x32_bf16 v[92:95], a[48:51], a[92:95], v[92:95]\n"
      "ds_read_b128 a[32:35], v47 offset:8192\n"
      "v_cvt_pk_bf16_f32 v232, v232, v233\n"
      "v_mfma_f32_16x16x32_bf16 v[124:127], a[52:55], a[92:95], v[124:127]\n"
      "buffer_load_dwordx4 v34, s[64:67], s28 offen offset:1024 sc1 lds\n"
      "v_cvt_pk_bf16_f32 v233, v234, v235\n"
      "v_mfma_f32_16x16x32_bf16 v[120:123], a[52:55], a[88:91], v[120:123]\n"
      "ds_read_b128 a[36:39], v47 offset:10240\n"
      "v_cvt_pk_bf16_f32 v234, v236, v237\n"
      "v_mfma_f32_16x16x32_bf16 v[116:119], a[52:55], a[84:87], v[116:119]\n"
      "ds_read_b128 a[40:43], v47 offset:12288\n"
      "v_cvt_pk_bf16_f32 v235, v238, v239\n"
      "v_mfma_f32_16x16x32_bf16 v[112:115], a[52:55], a[80:83], v[112:115]\n"
      "ds_read_b128 a[44:47], v47 offset:14336\n"
      "v_pk_max_i16 v232, v232, 0\n"
      "v_mfma_f32_16x16x32_bf16 v[108:111], a[52:55], a[76:79], v[108:111]\n"
      "ds_read_b128 a[4:7], v45 offset:34816\n"
      "v_pk_max_i16 v234, v234, 0\n"
      "v_mfma_f32_16x16x32_bf16 v[104:107], a[52:55], a[72:75], v[104:107]\n"
      "buffer_load_dwordx4 v35, s[64:67], s28 offen offset:2048 sc1 lds\n"
      "v_pk_max_i16 v233, v233, 0\n"
      "v_mfma_f32_16x16x32_bf16 v[100:103], a[52:55], a[68:71], v[100:103]\n"
      "ds_read_b128 a[8:11], v45 offset:36864\n"
      "v_pk_max_i16 v235, v235, 0\n"
      "v_mfma_f32_16x16x32_bf16 v[96:99], a[52:55], a[64:67], v[96:99]\n"
      "ds_read_b128 a[12:15], v45 offset:38912\n"
      "v_permlane16_swap_b32 v232, v234\n"
      "v_mfma_f32_16x16x32_bf16 v[128:131], a[56:59], a[64:67], v[128:131]\n"
      "s_nop 0\n"
      "v_permlane16_swap_b32 v233, v235\n"
      "v_mfma_f32_16x16x32_bf16 v[132:135], a[56:59], a[68:71], v[132:135]\n"
      "buffer_store_dwordx4 v[232:235], v13, s[64:67], s75 offen sc1\n"
      "v_accvgpr_read_b32 v240, a144\n"
      "v_mfma_f32_16x16x32_bf16 v[136:139], a[56:59], a[72:75], v[136:139]\n"
      "buffer_load_dwordx4 v36, s[64:67], s28 offen offset:3072 sc1 lds\n"
      "v_accvgpr_read_b32 v241, a145\n"
      "v_mfma_f32_16x16x32_bf16 v[140:143], a[56:59], a[76:79], v[140:143]\n"
      "s_and_b32 s83, s10, 15\n"
      "v_accvgpr_read_b32 v242, a146\n"
      "v_mfma_f32_16x16x32_bf16 v[144:147], a[56:59], a[80:83], v[144:147]\n"
      "s_lshr_b64 s[10:11], s[10:11], 4\n"
      "v_accvgpr_read_b32 v243, a147\n"
      "v_mfma_f32_16x16x32_bf16 v[148:151], a[56:59], a[84:87], v[148:151]\n"
      "s_mul_i32 s28, s83, s30\n"
      "v_accvgpr_read_b32 v244, a148\n"
      "v_mfma_f32_16x16x32_bf16 v[152:155], a[56:59], a[88:91], v[152:155]\n"
      "s_add_u32 s28, s28, s81\n"
      "s_add_u32 m0, s96, 0\n"
      "v_mfma_f32_16x16x32_bf16 v[156:159], a[56:59], a[92:95], v[156:159]\n"
      "buffer_load_dwordx4 v37, s[24:27], s29 offen offset:0 lds\n"
      "v_accvgpr_read_b32 v245, a149\n"
      "v_mfma_f32_16x16x32_bf16 v[188:191], a[60:63], a[92:95], v[188:191]\n"
      "v_accvgpr_read_b32 v246, a150\n"
      "v_accvgpr_read_b32 v247, a151\n"
      "v_mfma_f32_16x16x32_bf16 v[184:187], a[60:63], a[88:91], v[184:187]\n"
      "v_cvt_pk_bf16_f32 v240, v240, v241\n"
      "v_cvt_pk_bf16_f32 v241, v242, v243\n"
      "v_mfma_f32_16x16x32_bf16 v[180:183], a[60:63], a[84:87], v[180:183]\n"
      "v_cvt_pk_bf16_f32 v242, v244, v245\n"
      "v_cvt_pk_bf16_f32 v243, v246, v247\n"
      "v_mfma_f32_16x16x32_bf16 v[176:179], a[60:63], a[80:83], v[176:179]\n"
      "v_pk_max_i16 v240, v240, 0\n"
      "v_pk_max_i16 v242, v242, 0\n"
      "v_mfma_f32_16x16x32_bf16 v[172:175], a[60:63], a[76:79], v[172:175]\n"
      "buffer_load_dwordx4 v38, s[24:27], s29 offen offset:1024 lds\n"
      "v_pk_max_i16 v241, v241, 0\n"
      "v_mfma_f32_16x16x32_bf16 v[168:171], a[60:63], a[72:75], v[168:171]\n"
      "v_pk_max_i16 v243, v243, 0\n"
      "v_permlane16_swap_b32 v240, v242\n"
      "v_mfma_f32_16x16x32_bf16 v[164:167], a[60:63], a[68:71], v[164:167]\n"
      "s_nop 0\n"
      "v_permlane16_swap_b32 v241, v243\n"
      "v_mfma_f32_16x16x32_bf16 v[160:163], a[60:63], a[64:67], v[160:163]\n"
      "buffer_store_dwordx4 v[240:243], v14, s[64:67], s75 offen sc1\n"
      "v_accvgpr_read_b32 v248, a152\n"
      "s_waitcnt lgkmcnt(0)\n"
      "v_mfma_f32_16x16x32_bf16 v[64:67], a[0:3], a[16:19], v[64:67]\n"
      "ds_read_b128 a[48:51], v46 offset:32768\n"
      "v_accvgpr_read_b32 v249, a153\n"
      "v_mfma_f32_16x16x32_bf16 v[68:71], a[0:3], a[20:23], v[68:71]\n"
      "ds_read_b128 a[64:67], v48 offset:0\n"
      "v_accvgpr_read_b32 v250, a154\n"
      "v_mfma_f32_16x16x32_bf16 v[72:75], a[0:3], a[24:27], v[72:75]\n"
      "ds_read_b128 a[68:71], v48 offset:2048\n"
      "v_accvgpr_read_b32 v251, a155\n"
      "v_mfma_f32_16x16x32_bf16 v[76:79], a[0:3], a[28:31], v[76:79]\n"
      "buffer_load_dwordx4 v39, s[24:27], s29 offen offset:2048 lds\n"
      "v_accvgpr_read_b32 v252, a156\n"
      "v_mfma_f32_16x16x32_bf16 v[80:83], a[0:3], a[32:35], v[80:83]\n"
      "ds_read_b128 a[72:75], v48 offset:4096\n"
      "v_accvgpr_read_b32 v253, a157\n"
      "v_mfma_f32_16x16x32_bf16 v[84:87], a[0:3], a[36:39], v[84:87]\n"
      "ds_read_b128 a[76:79], v48 offset:6144\n"
      "v_accvgpr_read_b32 v254, a158\n"
      "v_mfma_f32_16x16x32_bf16 v[88:91], a[0:3], a[40:43], v[88:91]\n"
      "ds_read_b128 a[80:83], v48 offset:8192\n"
      "v_accvgpr_read_b32 v255, a159\n"
      "v_mfma_f32_16x16x32_bf16 v[92:95], a[0:3], a[44:47], v[92:95]\n"
      "ds_read_b128 a[84:87], v48 offset:10240\n"
      "v_cvt_pk_bf16_f32 v248, v248, v249\n"
      "v_mfma_f32_16x16x32_bf16 v[124:127], a[4:7], a[44:47], v[124:127]\n"
      "buffer_load_dwordx4 v40, s[24:27], s29 offen offset:3072 lds\n"
      "v_cvt_pk_bf16_f32 v249, v250, v251\n"
      "v_mfma_f32_16x16x32_bf16 v[120:123], a[4:7], a[40:43], v[120:123]\n"
      "ds_read_b128 a[88:91], v48 offset:12288\n"
      "v_cvt_pk_bf16_f32 v250, v252, v253\n"
      "v_mfma_f32_16x16x32_bf16 v[116:119], a[4:7], a[36:39], v[116:119]\n"
      "ds_read_b128 a[92:95], v48 offset:14336\n"
      "v_cvt_pk_bf16_f32 v251, v254, v255\n"
      "v_mfma_f32_16x16x32_bf16 v[112:115], a[4:7], a[32:35], v[112:115]\n"
      "ds_read_b128 a[52:55], v46 offset:34816\n"
      "v_pk_max_i16 v248, v248, 0\n"
      "v_mfma_f32_16x16x32_bf16 v[108:111], a[4:7], a[28:31], v[108:111]\n"
      "ds_read_b128 a[56:59], v46 offset:36864\n"
      "s_add_u32 m0, s96, 4096\n"
      "v_mfma_f32_16x16x32_bf16 v[104:107], a[4:7], a[24:27], v[104:107]\n"
      "buffer_load_dwordx4 v41, s[24:27], s29 offen offset:0 lds\n"
      "v_pk_max_i16 v250, v250, 0\n"
      "v_mfma_f32_16x16x32_bf16 v[100:103], a[4:7], a[20:23], v[100:103]\n"
      "ds_read_b128 a[60:63], v46 offset:38912\n"
      "v_pk_max_i16 v249, v249, 0\n"
      "v_mfma_f32_16x16x32_bf16 v[96:99], a[4:7], a[16:19], v[96:99]\n"
      "s_add_u32 s96, s94, s85\n"
      "v_pk_max_i16 v251, v251, 0\n"
      "v_mfma_f32_16x16x32_bf16 v[128:131], a[8:11], a[16:19], v[128:131]\n"
      "s_add_u32 s95, s94, s92\n"
      "s_add_u32 s95, s95, 32768\n"
      "v_mfma_f32_16x16x32_bf16 v[132:135], a[8:11], a[20:23], v[132:135]\n"
      "s_cmp_eq_u32 s94, 98304\n"
      "s_cselect_b32 s97, 0xfffe8000, s99\n"
      "s_add_u32 s94, s94, s97\n"
      "v_mfma_f32_16x16x32_bf16 v[136:139], a[8:11], a[24:27], v[136:139]\n"
      "buffer_load_dwordx4 v42, s[24:27], s29 offen offset:1024 lds\n"
      "v_permlane16_swap_b32 v248, v250\n"
      "v_mfma_f32_16x16x32_bf16 v[140:143], a[8:11], a[28:31], v[140:143]\n"
      "v_add_u32 v45, s97, v45\n"
      "v_add_u32 v46, s97, v46\n"
      "v_mfma_f32_16x16x32_bf16 v[144:147], a[8:11], a[32:35], v[144:147]\n"
      "v_add_u32 v47, s97, v47\n"
      "v_add_u32 v48, s97, v48\n"
      "v_mfma_f32_16x16x32_bf16 v[148:151], a[8:11], a[36:39], v[148:151]\n"
      "s_nop 0\n"
      "v_permlane16_swap_b32 v249, v251\n"
      "v_mfma_f32_16x16x32_bf16 v[152:155], a[8:11], a[40:43], v[152:155]\n"
      "buffer_store_dwordx4 v[248:251], v15, s[64:67], s75 offen sc1\n"
      "v_mfma_f32_16x16x32_bf16 v[156:159], a[8:11], a[44:47], v[156:159]\n"
      "buffer_load_dwordx4 v43, s[24:27], s29 offen offset:2048 lds\n"
      "v_mfma_f32_16x16x32_bf16 v[188:191], a[12:15], a[44:47], v[188:191]\n"
      "v_mfma_f32_16x16x32_bf16 v[184:187], a[12:15], a[40:43], v[184:187]\n"
      "v_mfma_f32_16x16x32_bf16 v[180:183], a[12:15], a[36:39], v[180:183]\n"
      "v_mfma_f32_16x16x32_bf16 v[176:179], a[12:15], a[32:35], v[176:179]\n"
      "v_mfma_f32_16x16x32_bf16 v[172:175], a[12:15], a[28:31], v[172:175]\n"
      "buffer_load_dwordx4 v44, s[24:27], s29 offen offset:3072 lds\n"
      "v_mfma_f32_16x16x32_bf16 v[168:171], a[12:15], a[24:27], v[168:171]\n"
      "s_lshl_b32 s29, s83, 7\n"
      "s_add_u32 s29, s29, s82\n"
      "v_mfma_f32_16x16x32_bf16 v[164:167], a[12:15], a[20:23], v[164:167]\n"
      "v_mfma_f32_16x16x32_bf16 v[160:163], a[12:15], a[16:19], v[160:163]\n"
      "s_waitcnt lgkmcnt(0)\n"
      "v_mfma_f32_16x16x32_bf16 v[64:67], a[48:51], a[64:67], v[64:67]\n"
      "s_waitcnt vmcnt(16)\n"
      "s_barrier\n"
      "v_mfma_f32_16x16x32_bf16 v[68:71], a[48:51], a[68:71], v[68:71]\n"
      "ds_read_b128 a[0:3], v45 offset:32768\n"
      "v_mfma_f32_16x16x32_bf16 v[72:75], a[48:51], a[72:75], v[72:75]\n"
      "ds_read_b128 a[16:19], v47 offset:0\n"
      "s_mov_b32 m0, s95\n"
      "v_mfma_f32_16x16x32_bf16 v[76:79], a[48:51], a[76:79], v[76:79]\n"
      "buffer_load_dwordx4 v33, s[64:67], s28 offen offset:0 sc1 lds\n"
      "v_mfma_f32_16x16x32_bf16 v[80:83], a[48:51], a[80:83], v[80:83]\n"
      "ds_read_b128 a[20:23], v47 offset:2048\n"
      "v_mfma_f32_16x16x32_bf16 v[84:87], a[48:51], a[84:87], v[84:87]\n"
      "ds_read_b128 a[24:27], v47 offset:4096\n"
      "v_mfma_f32_16x16x32_bf16 v[88:91], a[48:51], a[88:91], v[88:91]\n"
      "ds_read_b128 a[28:31], v47 offset:6144\n"
      "v_mfma_f32_16x16x32_bf16 v[92:95], a[48:51], a[92:95], v[92:95]\n"
      "ds_read_b128 a[32:35], v47 offset:8192\n"
      "v_mfma_f32_16x16x32_bf16 v[124:127], a[52:55], a[92:95], v[124:127]\n"
      "buffer_load_dwordx4 v34, s[64:67], s28 offen offset:1024 sc1 lds\n"
      "v_mfma_f32_16x16x32_bf16 v[120:123], a[52:55], a[88:91], v[120:123]\n"
      "ds_read_b128 a[36:39], v47 offset:10240\n"
      "v_mfma_f32_16x16x32_bf16 v[116:119], a[52:55], a[84:87], v[116:119]\n"
      "ds_read_b128 a[40:43], v47 offset:12288\n"
      "v_mfma_f32_16x16x32_bf16 v[112:115], a[52:55], a[80:83], v[112:115]\n"
      "ds_read_b128 a[44:47], v47 offset:14336\n"
      "v_mfma_f32_16x16x32_bf16 v[108:111], a[52:55], a[76:79], v[108:111]\n"
      "ds_read_b128 a[4:7], v45 offset:34816\n"
      "v_mfma_f32_16x16x32_bf16 v[104:107], a[52:55], a[72:75], v[104:107]\n"
      "buffer_load_dwordx4 v35, s[64:67], s28 offen offset:2048 sc1 lds\n"
      "v_mfma_f32_16x16x32_bf16 v[100:103], a[52:55], a[68:71], v[100:103]\n"
      "ds_read_b128 a[8:11], v45 offset:36864\n"
      "v_mfma_f32_16x16x32_bf16 v[96:99], a[52:55], a[64:67], v[96:99]\n"
      "ds_read_b128 a[12:15], v45 offset:38912\n"
      "v_mfma_f32_16x16x32_bf16 v[128:131], a[56:59], a[64:67], v[128:131]\n"
      "v_mfma_f32_16x16x32_bf16 v[132:135], a[56:59], a[68:71], v[132:135]\n"
      "v_mfma_f32_16x16x32_bf16 v[136:139], a[56:59], a[72:75], v[136:139]\n"
      "buffer_load_dwordx4 v36, s[64:67], s28 offen offset:3072 sc1 lds\n"
      "v_mfma_f32_16x16x32_bf16 v[140:143], a[56:59], a[76:79], v[140:143]\n"
      "s_and_b32 s83, s10, 15\n"
      "v_mfma_f32_16x16x32_bf16 v[144:147], a[56:59], a[80:83], v[144:147]\n"
      "s_lshr_b64 s[10:11], s[10:11], 4\n"
      "v_mfma_f32_16x16x32_bf16 v[148:151], a[56:59], a[84:87], v[148:151]\n"
      "s_mul_i32 s28, s83, s30\n"
      "v_mfma_f32_16x16x32_bf16 v[152:155], a[56:59], a[88:91], v[152:155]\n"
      "s_add_u32 s28, s28, s81\n"
      "s_add_u32 m0, s96, 0\n"
      "v_mfma_f32_16x16x32_bf16 v[156:159], a[56:59], a[92:95], v[156:159]\n"
      "buffer_load_dwordx4 v37, s[24:27], s29 offen offset:0 lds\n"
      "v_mfma_f32_16x16x32_bf16 v[188:191], a[60:63], a[92:95], v[188:191]\n"
      "v_mfma_f32_16x16x32_bf16 v[184:187], a[60:63], a[88:91], v[184:187]\n"
      "v_mfma_f32_16x16x32_bf16 v[180:183], a[60:63], a[84:87], v[180:183]\n"
      "v_mfma_f32_16x16x32_bf16 v[176:179], a[60:63], a[80:83], v[176:179]\n"
      "v_mfma_f32_16x16x32_bf16 v[172:175], a[60:63], a[76:79], v[172:175]\n"
      "buffer_load_dwordx4 v38, s[24:27], s29 offen offset:1024 lds\n"
      "v_mfma_f32_16x16x32_bf16 v[168:171], a[60:63], a[72:75], v[168:171]\n"
      "v_mfma_f32_16x16x32_bf16 v[164:167], a[60:63], a[68:71], v[164:167]\n"
      "v_mfma_f32_16x16x32_bf16 v[160:163], a[60:63], a[64:67], v[160:163]\n"
      "s_waitcnt lgkmcnt(0)\n"
      "v_mfma_f32_16x16x32_bf16 v[64:67], a[0:3], a[16:19], v[64:67]\n"
      "ds_read_b128 a[48:51], v46 offset:32768\n"
      "v_mfma_f32_16x16x32_bf16 v[68:71], a[0:3], a[20:23], v[68:71]\n"
      "ds_read_b128 a[64:67], v48 offset:0\n"
      "v_mfma_f32_16x16x32_bf16 v[72:75], a[0:3], a[24:27], v[72:75]\n"
      "ds_read_b128 a[68:71], v48 offset:2048\n"
      "v_mfma_f32_16x16x32_bf16 v[76:79], a[0:3], a[28:31], v[76:79]\n"
      "buffer_load_dwordx4 v39, s[24:27], s29 offen offset:2048 lds\n"
      "v_mfma_f32_16x16x32_bf16 v[80:83], a[0:3], a[32:35], v[80:83]\n"
      "ds_read_b128 a[72:75], v48 offset:4096\n"
      "v_mfma_f32_16x16x32_bf16 v[84:87], a[0:3], a[36:39], v[84:87]\n"
      "ds_read_b128 a[76:79], v48 offset:6144\n"
      "v_mfma_f32_16x16x32_bf16 v[88:91], a[0:3], a[40:43], v[88:91]\n"
      "ds_read_b128 a[80:83], v48 offset:8192\n"
      "v_mfma_f32_16x16x32_bf16 v[92:95], a[0:3], a[44:47], v[92:95]\n"
      "ds_read_b128 a[84:87], v48 offset:10240\n"
      "v_mfma_f32_16x16x32_bf16 v[124:127], a[4:7], a[44:47], v[124:127]\n"
      "buffer_load_dwordx4 v40, s[24:27], s29 offen offset:3072 lds\n"
      "v_mfma_f32_16x16x32_bf16 v[120:123], a[4:7], a[40:43], v[120:123]\n"
      "ds_read_b128 a[88:91], v48 offset:12288\n"
      "v_mfma_f32_16x16x32_bf16 v[116:119], a[4:7], a[36:39], v[116:119]\n"
      "ds_read_b128 a[92:95], v48 offset:14336\n"
      "v_mfma_f32_16x16x32_bf16 v[112:115], a[4:7], a[32:35], v[112:115]\n"
      "ds_read_b128 a[52:55], v46 offset:34816\n"
      "v_mfma_f32_16x16x32_bf16 v[108:111], a[4:7], a[28:31], v[108:111]\n"
      "ds_read_b128 a[56:59], v46 offset:36864\n"
      "s_add_u32 m0, s96, 4096\n"
      "v_mfma_f32_16x16x32_bf16 v[104:107], a[4:7], a[24:27], v[104:107]\n"
      "buffer_load_dwordx4 v41, s[24:27], s29 offen offset:0 lds\n"
      "v_mfma_f32_16x16x32_bf16 v[100:103], a[4:7], a[20:23], v[100:103]\n"
      "ds_read_b128 a[60:63], v46 offset:38912\n"
      "v_mfma_f32_16x16x32_bf16 v[96:99], a[4:7], a[16:19], v[96:99]\n"
      "s_add_u32 s96, s94, s85\n"
      "v_mfma_f32_16x16x32_bf16 v[128:131], a[8:11], a[16:19], v[128:131]\n"
      "s_add_u32 s95, s94, s92\n"
      "s_add_u32 s95, s95, 32768\n"
      "v_mfma_f32_16x16x32_bf16 v[132:135], a[8:11], a[20:23], v[132:135]\n"
      "s_cmp_eq_u32 s94, 98304\n"
      "s_cselect_b32 s97, 0xfffe8000, s99\n"
      "s_add_u32 s94, s94, s97\n"
      "v_mfma_f32_16x16x32_bf16 v[136:139], a[8:11], a[24:27], v[136:139]\n"
      "buffer_load_dwordx4 v42, s[24:27], s29 offen offset:1024 lds\n"
      "v_mfma_f32_16x16x32_bf16 v[140:143], a[8:11], a[28:31], v[140:143]\n"
      "v_add_u32 v45, s97, v45\n"
      "v_add_u32 v46, s97, v46\n"
      "v_mfma_f32_16x16x32_bf16 v[144:147], a[8:11], a[32:35], v[144:147]\n"
      "v_add_u32 v47, s97, v47\n"
      "v_add_u32 v48, s97, v48\n"
      "v_mfma_f32_16x16x32_bf16 v[148:151], a[8:11], a[36:39], v[148:151]\n"
      "v_mfma_f32_16x16x32_bf16 v[152:155], a[8:11], a[40:43], v[152:155]\n"
      "v_mfma_f32_16x16x32_bf16 v[156:159], a[8:11], a[44:47], v[156:159]\n"
      "buffer_load_dwordx4 v43, s[24:27], s29 offen offset:2048 lds\n"
      "v_mfma_f32_16x16x32_bf16 v[188:191], a[12:15], a[44:47], v[188:191]\n"
      "v_mfma_f32_16x16x32_bf16 v[184:187], a[12:15], a[40:43], v[184:187]\n"
      "v_mfma_f32_16x16x32_bf16 v[180:183], a[12:15], a[36:39], v[180:183]\n"
      "v_mfma_f32_16x16x32_bf16 v[176:179], a[12:15], a[32:35], v[176:179]\n"
      "v_mfma_f32_16x16x32_bf16 v[172:175], a[12:15], a[28:31], v[172:175]\n"
      "buffer_load_dwordx4 v44, s[24:27], s29 offen offset:3072 lds\n"
      "v_mfma_f32_16x16x32_bf16 v[168:171], a[12:15], a[24:27], v[168:171]\n"
      "s_lshl_b32 s29, s83, 7\n"
      "s_add_u32 s29, s29, s82\n"
      "v_mfma_f32_16x16x32_bf16 v[164:167], a[12:15], a[20:23], v[164:167]\n"
      "v_mfma_f32_16x16x32_bf16 v[160:163], a[12:15], a[16:19], v[160:163]\n"
      "s_waitcnt vmcnt(12) lgkmcnt(0)\n"
      "s_barrier\n"
      "s_lshl_b32 s72, s59, 11\n"
      "s_lshl_b32 s73, s60, 7\n"
      "s_add_u32 s72, s72, s73\n"
      "s_add_u32 s72, s72, 180224\n"
      "s_cmp_eq_u32 s61, 0\n"
      "s_cbranch_scc0 Lk_pub_7\n"
      "v_mov_b32 v6, s54\n"
      "v_mov_b32 v7, s55\n"
      "v_min_u32 v5, 1, v4\n"
      "v_lshlrev_b32 v5, 30, v5\n"
      "buffer_store_dwordx2 v[6:7], v5, s[64:67], s72 offen sc1\n"
      "Lk_pub_7:\n"
      "v_mfma_f32_16x16x32_bf16 v[64:67], a[48:51], a[64:67], v[64:67]\n"
      "ds_read_b128 a[0:3], v45 offset:32768\n"
      "v_mfma_f32_16x16x32_bf16 v[68:71], a[48:51], a[68:71], v[68:71]\n"
      "ds_read_b128 a[16:19], v47 offset:0\n"
      "v_mfma_f32_16x16x32_bf16 v[72:75], a[48:51], a[72:75], v[72:75]\n"
      "ds_read_b128 a[20:23], v47 offset:2048\n"
      "s_mov_b32 m0, s95\n"
      "v_mfma_f32_16x16x32_bf16 v[76:79], a[48:51], a[76:79], v[76:79]\n"
      "buffer_load_dwordx4 v33, s[64:67], s28 offen offset:0 sc1 lds\n"
      "v_mfma_f32_16x16x32_bf16 v[80:83], a[48:51], a[80:83], v[80:83]\n"
      "ds_read_b128 a[24:27], v47 offset:4096\n"
      "v_mfma_f32_16x16x32_bf16 v[84:87], a[48:51], a[84:87], v[84:87]\n"
      "ds_read_b128 a[28:31], v47 offset:6144\n"
      "v_mfma_f32_16x16x32_bf16 v[88:91], a[48:51], a[88:91], v[88:91]\n"
      "ds_read_b128 a[32:35], v47 offset:8192\n"
      "v_mfma_f32_16x16x32_bf16 v[92:95], a[48:51], a[92:95], v[92:95]\n"
      "ds_read_b128 a[36:39], v47 offset:10240\n"
      "v_mfma_f32_16x16x32_bf16 v[124:127], a[52:55], a[92:95], v[124:127]\n"
      "buffer_load_dwordx4 v34, s[64:67], s28 offen offset:1024 sc1 lds\n"
      "v_mfma_f32_16x16x32_bf16 v[120:123], a[52:55], a[88:91], v[120:123]\n"
      "ds_read_b128 a[40:43], v47 offset:12288\n"
      "v_mfma_f32_16x16x32_bf16 v[116:119], a[52:55], a[84:87], v[116:119]\n"
      "ds_read_b128 a[44:47], v47 offset:14336\n"
      "v_mfma_f32_16x16x32_bf16 v[112:115], a[52:55], a[80:83], v[112:115]\n"
      "ds_read_b128 a[4:7], v45 offset:34816\n"
      "v_mfma_f32_16x16x32_bf16 v[108:111], a[52:55], a[76:79], v[108:111]\n"
      "ds_read_b128 a[8:11], v45 offset:36864\n"
      "v_mfma_f32_16x16x32_bf16 v[104:107], a[52:55], a[72:75], v[104:107]\n"
      "buffer_load_dwordx4 v35, s[64:67], s28 offen offset:2048 sc1 lds\n"
      "v_mfma_f32_16x16x32_bf16 v[100:103], a[52:55], a[68:71], v[100:103]\n"
      "ds_read_b128 a[12:15], v45 offset:38912\n"
      "v_mfma_f32_16x16x32_bf16 v[96:99], a[52:55], a[64:67], v[96:99]\n"
      "v_mfma_f32_16x16x32_bf16 v[128:131], a[56:59], a[64:67], v[128:131]\n"
      "v_mfma_f32_16x16x32_bf16 v[132:135], a[56:59], a[68:71], v[132:135]\n"
      "v_mfma_f32_16x16x32_bf16 v[136:139], a[56:59], a[72:75], v[136:139]\n"
      "buffer_load_dwordx4 v36, s[64:67], s28 offen offset:3072 sc1 lds\n"
      "v_mfma_f32_16x16x32_bf16 v[140:143], a[56:59], a[76:79], v[140:143]\n"
      "s_and_b32 s83, s10, 15\n"
      "v_mfma_f32_16x16x32_bf16 v[144:147], a[56:59], a[80:83], v[144:147]\n"
      "s_lshr_b64 s[10:11], s[10:11], 4\n"
      "v_mfma_f32_16x16x32_bf16 v[148:151], a[56:59], a[84:87], v[148:151]\n"
      "s_mul_i32 s28, s83, s30\n"
      "v_mfma_f32_16x16x32_bf16 v[152:155], a[56:59], a[88:91], v[152:155]\n"
      "s_add_u32 s28, s28, s81\n"
      "s_add_u32 m0, s96, 0\n"
      "v_mfma_f32_16x16x32_bf16 v[156:159], a[56:59], a[92:95], v[156:159]\n"
      "buffer_load_dwordx4 v37, s[24:27], s29 offen offset:0 lds\n"
      "v_mfma_f32_16x16x32_bf16 v[188:191], a[60:63], a[92:95], v[188:191]\n"
      "v_mfma_f32_16x16x32_bf16 v[184:187], a[60:63], a[88:91], v[184:187]\n"
      "v_mfma_f32_16x16x32_bf16 v[180:183], a[60:63], a[84:87], v[180:183]\n"
      "v_mfma_f32_16x16x32_bf16 v[176:179], a[60:63], a[80:83], v[176:179]\n"
      "v_mfma_f32_16x16x32_bf16 v[172:175], a[60:63], a[76:79], v[172:175]\n"
      "buffer_load_dwordx4 v38, s[24:27], s29 offen offset:1024 lds\n"
      "v_mfma_f32_16x16x32_bf16 v[168:171], a[60:63], a[72:75], v[168:171]\n"
      "v_mfma_f32_16x16x32_bf16 v[164:167], a[60:63], a[68:71], v[164:167]\n"
      "v_mfma_f32_16x16x32_bf16 v[160:163], a[60:63], a[64:67], v[160:163]\n"
      "s_waitcnt lgkmcnt(0)\n"
      "v_mfma_f32_16x16x32_bf16 v[64:67], a[0:3], a[16:19], v[64:67]\n"
      "ds_read_b128 a[48:51], v46 offset:32768\n"
      "v_mfma_f32_16x16x32_bf16 v[68:71], a[0:3], a[20:23], v[68:71]\n"
      "ds_read_b128 a[64:67], v48 offset:0\n"
      "v_mfma_f32_16x16x32_bf16 v[72:75], a[0:3], a[24:27], v[72:75]\n"
      "ds_read_b128 a[68:71], v48 offset:2048\n"
      "v_mfma_f32_16x16x32_bf16 v[76:79], a[0:3], a[28:31], v[76:79]\n"
      "buffer_load_dwordx4 v39, s[24:27], s29 offen offset:2048 lds\n"
      "v_mfma_f32_16x16x32_bf16 v[80:83], a[0:3], a[32:35], v[80:83]\n"
      "ds_read_b128 a[72:75], v48 offset:4096\n"
      "v_mfma_f32_16x16x32_bf16 v[84:87], a[0:3], a[36:39], v[84:87]\n"
      "ds_read_b128 a[76:79], v48 offset:6144\n"
      "v_mfma_f32_16x16x32_bf16 v[88:91], a[0:3], a[40:43], v[88:91]\n"
      "ds_read_b128 a[80:83], v48 offset:8192\n"
      "v_mfma_f32_16x16x32_bf16 v[92:95], a[0:3], a[44:47], v[92:95]\n"
      "ds_read_b128 a[84:87], v48 offset:10240\n"
      "v_mfma_f32_16x16x32_bf16 v[124:127], a[4:7], a[44:47], v[124:127]\n"
      "buffer_load_dwordx4 v40, s[24:27], s29 offen offset:3072 lds\n"
      "v_mfma_f32_16x16x32_bf16 v[120:123], a[4:7], a[40:43], v[120:123]\n"
      "ds_read_b128 a[88:91], v48 offset:12288\n"
      "v_mfma_f32_16x16x32_bf16 v[116:119], a[4:7], a[36:39], v[116:119]\n"
      "ds_read_b128 a[92:95], v48 offset:14336\n"
      "v_mfma_f32_16x16x32_bf16 v[112:115], a[4:7], a[32:35], v[112:115]\n"
      "ds_read_b128 a[52:55], v46 offset:34816\n"
      "v_mfma_f32_16x16x32_bf16 v[108:111], a[4:7], a[28:31], v[108:111]\n"
      "ds_read_b128 a[56:59], v46 offset:36864\n"
      "s_add_u32 m0, s96, 4096\n"
      "v_mfma_f32_16x16x32_bf16 v[104:107], a[4:7], a[24:27], v[104:107]\n"
      "buffer_load_dwordx4 v41, s[24:27], s29 offen offset:0 lds\n"
      "v_mfma_f32_16x16x32_bf16 v[100:103], a[4:7], a[20:23], v[100:103]\n"
      "ds_read_b128 a[60:63], v46 offset:38912\n"
      "v_mfma_f32_16x16x32_bf16 v[96:99], a[4:7], a[16:19], v[96:99]\n"
      "s_add_u32 s96, s94, s85\n"
      "v_mfma_f32_16x16x32_bf16 v[128:131], a[8:11], a[16:19], v[128:131]\n"
      "s_add_u32 s95, s94, s92\n"
      "s_add_u32 s95, s95, 32768\n"
      "v_mfma_f32_16x16x32_bf16 v[132:135], a[8:11], a[20:23], v[132:135]\n"
      "s_cmp_eq_u32 s94, 98304\n"
      "s_cselect_b32 s97, 0xfffe8000, s99\n"
      "s_add_u32 s94, s94, s97\n"
      "v_mfma_f32_16x16x32_bf16 v[136:139], a[8:11], a[24:27], v[136:139]\n"
      "buffer_load_dwordx4 v42, s[24:27], s29 offen offset:1024 lds\n"
      "v_mfma_f32_16x16x32_bf16 v[140:143], a[8:11], a[28:31], v[140:143]\n"
      "v_add_u32 v45, s97, v45\n"
      "v_add_u32 v46, s97, v46\n"
      "v_mfma_f32_16x16x32_bf16 v[144:147], a[8:11], a[32:35], v[144:147]\n"
      "v_add_u32 v47, s97, v47\n"
      "v_add_u32 v48, s97, v48\n"
      "v_mfma_f32_16x16x32_bf16 v[148:151], a[8:11], a[36:39], v[148:151]\n"
      "v_mfma_f32_16x16x32_bf16 v[152:155], a[8:11], a[40:43], v[152:155]\n"
      "v_mfma_f32_16x16x32_bf16 v[156:159], a[8:11], a[44:47], v[156:159]\n"
      "buffer_load_dwordx4 v43, s[24:27], s29 offen offset:2048 lds\n"
      "v_mfma_f32_16x16x32_bf16 v[188:191], a[12:15], a[44:47], v[188:191]\n"
      "v_mfma_f32_16x16x32_bf16 v[184:187], a[12:15], a[40:43], v[184:187]\n"
      "v_mfma_f32_16x16x32_bf16 v[180:183], a[12:15], a[36:39], v[180:183]\n"
      "v_mfma_f32_16x16x32_bf16 v[176:179], a[12:15], a[32:35], v[176:179]\n"
      "v_mfma_f32_16x16x32_bf16 v[172:175], a[12:15], a[28:31], v[172:175]\n"
      "buffer_load_dwordx4 v44, s[24:27], s29 offen offset:3072 lds\n"
      "v_mfma_f32_16x16x32_bf16 v[168:171], a[12:15], a[24:27], v[168:171]\n"
      "s_lshl_b32 s29, s83, 7\n"
      "s_add_u32 s29, s29, s82\n"
      "v_mfma_f32_16x16x32_bf16 v[164:167], a[12:15], a[20:23], v[164:167]\n"
      "v_mfma_f32_16x16x32_bf16 v[160:163], a[12:15], a[16:19], v[160:163]\n"
      "s_lshl_b32 s80, s57, 11\n"
      "s_add_u32 s80, s80, 180224\n"
      "s_mov_b32 s93, 1\n"
      "s_call_b64 s[34:35], Lk_nt\n"
      "s_mov_b32 s84, 0x40000\n"
      "Lk_pl_9:\n"
      "buffer_load_dwordx2 v[56:57], v55, s[64:67], s80 offen sc1\n"
      "s_waitcnt vmcnt(0)\n"
      "v_cmp_ne_u64 vcc, s[54:55], v[56:57]\n"
      "s_cbranch_vccz Lk_pd_8\n"
      "s_sleep 1\n"
      "s_sub_u32 s84, s84, 1\n"
      "s_cmp_lg_u32 s84, 0\n"
      "s_cbranch_scc1 Lk_pl_9\n"
      "Lk_pd_8:\n"
      "s_mov_b32 s93, 12\n"
      "s_branch Lk_ffn\n"
      "Lk_prog_d:\n"
      "s_lshl_b32 s72, s61, 5\n"
      "v_add_u32 v206, s72, v205\n"
      "v_lshlrev_b32 v207, 11, v206\n"
      "v_add_u32 v209, v207, v212\n"
      "v_add_u32 v210, v207, v213\n"
      "v_add_u32 v33, 0x0, v209\n"
      "v_add_u32 v34, 0x3c00, v210\n"
      "v_add_u32 v35, 0x7800, v209\n"
      "v_add_u32 v36, 0xb400, v210\n"
      "s_mov_b32 s20, s48\n"
      "s_and_b32 s21, s49, 0xffff\n"
      "s_mov_b32 s22, 0x700000\n"
      "s_mov_b32 s23, 0x20000\n"
      "v_accvgpr_read_b32 v23, a221\n"
      "v_accvgpr_read_b32 v24, a222\n"
      "s_and_b32 s83, s56, 1\n"
      "s_mul_i32 s83, s83, 13\n"
      "s_add_u32 s83, s83, 30\n"
      "s_lshl_b32 s91, s83, 17\n"
      "s_lshl_b32 s98, s58, 8\n"
      "s_add_u32 s91, s91, s98\n"
      "s_mul_i32 s98, s57, 56\n"
      "s_add_u32 s98, s98, s83\n"
      "s_lshl_b32 s98, s98, 15\n"
      "s_add_u32 s98, s98, 0x1000000\n"
      "s_mov_b32 s94, 0\n"
      "s_lshl_b32 s80, s5, 11\n"
      "s_add_u32 s80, s80, 131072\n"
      "v_and_b32 v55, 15, v4\n"
      "v_lshlrev_b32 v55, 7, v55\n"
      "v_bfe_u32 v5, v4, 4, 1\n"
      "v_bfe_u32 v6, v4, 5, 1\n"
      "v_and_b32 v7, 15, v4\n"
      "v_lshlrev_b32 v7, 4, v7\n"
      "v_lshl_add_u32 v7, v6, 8, v7\n"
      "v_lshl_add_u32 v58, v5, 9, v7\n"
      "s_lshl_b32 s72, s58, 6\n"
      "s_lshl_b32 s73, s5, 1\n"
      "s_add_u32 s72, s72, s73\n"
      "s_add_u32 s72, s72, s62\n"
      "s_lshl_b32 s72, s72, 14\n"
      "s_lshl_b32 s73, s63, 13\n"
      "s_add_u32 s72, s72, s73\n"
      "s_add_u32 s72, s72, 0x800000\n"
      "s_add_u32 s73, s72, 4096\n"
      "s_add_u32 s74, s72, 0x80000\n"
      "s_add_u32 s75, s74, 4096\n"
      "s_and_b32 s83, s56, 1\n"
      "s_mul_i32 s83, s83, 16\n"
      "s_lshl_b32 s76, s58, 6\n"
      "s_add_u32 s76, s76, s83\n"
      "s_lshl_b32 s76, s76, 14\n"
      "s_add_u32 s76, s76, 0x800000\n"
      "s_lshl_b32 s77, s57, 20\n"
      "s_lshl_b32 s83, s83, 7\n"
      "s_add_u32 s77, s77, s83\n"
      "s_and_b32 s83, s56, 1\n"
      "s_mul_i32 s83, s83, 7\n"
      "s_lshl_b32 s78, s58, 6\n"
      "s_add_u32 s78, s78, s83\n"
      "s_lshl_b32 s78, s78, 14\n"
      "s_add_u32 s78, s78, 0x880000\n"
      "s_lshl_b32 s79, s57, 20\n"
      "s_lshl_b32 s83, s83, 7\n"
      "s_add_u32 s79, s79, s83\n"
      "s_mov_b32 s99, 49152\n"
      "s_cmp_eq_u32 s61, 0\n"
      "s_cbranch_scc0 Lk_pw_10\n"
      "s_mov_b32 s84, 0x40000\n"
      "Lk_pl_12:\n"
      "buffer_load_dwordx2 v[56:57], v55, s[64:67], s80 offen sc1\n"
      "s_waitcnt vmcnt(0)\n"
      "v_cmp_ne_u64 vcc, s[54:55], v[56:57]\n"
      "s_cbranch_vccz Lk_pd_11\n"
      "s_sleep 1\n"
      "s_sub_u32 s84, s84, 1\n"
      "s_cmp_lg_u32 s84, 0\n"
      "s_cbranch_scc1 Lk_pl_12\n"
      "Lk_pd_11:\n"
      "Lk_pw_10:\n"
      "s_barrier\n"
      "s_add_u32 m0, s92, 32768\n"
      "s_nop 0\n"
      "buffer_load_dwordx4 v33, s[64:67], s28 offen offset:0 sc1 lds\n"
      "buffer_load_dwordx4 v34, s[64:67], s28 offen offset:1024 sc1 lds\n"
      "buffer_load_dwordx4 v35, s[64:67], s28 offen offset:2048 sc1 lds\n"
      "buffer_load_dwordx4 v36, s[64:67], s28 offen offset:3072 sc1 lds\n"
      "s_and_b32 s83, s10, 15\n"
      "s_lshr_b64 s[10:11], s[10:11], 4\n"
      "s_mul_i32 s28, s83, s30\n"
      "s_add_u32 s28, s28, s81\n"
      "s_lshl_b32 s29, s83, 7\n"
      "s_add_u32 s29, s29, s82\n"
      "s_add_u32 m0, s92, 81920\n"
      "s_nop 0\n"
      "buffer_load_dwordx4 v33, s[64:67], s28 offen offset:0 sc1 lds\n"
      "buffer_load_dwordx4 v34, s[64:67], s28 offen offset:1024 sc1 lds\n"
      "buffer_load_dwordx4 v35, s[64:67], s28 offen offset:2048 sc1 lds\n"
      "buffer_load_dwordx4 v36, s[64:67], s28 offen offset:3072 sc1 lds\n"
      "s_and_b32 s83, s10, 15\n"
      "s_lshr_b64 s[10:11], s[10:11], 4\n"
      "s_mul_i32 s28, s83, s30\n"
      "s_add_u32 s28, s28, s81\n"
      "s_add_u32 m0, s85, 49152\n"
      "s_nop 0\n"
      "buffer_load_dwordx4 v37, s[24:27], s29 offen offset:0 lds\n"
      "buffer_load_dwordx4 v38, s[24:27], s29 offen offset:1024 lds\n"
      "buffer_load_dwordx4 v39, s[24:27], s29 offen offset:2048 lds\n"
      "buffer_load_dwordx4 v40, s[24:27], s29 offen offset:3072 lds\n"
      "s_add_u32 m0, s85, 53248\n"
      "s_nop 0\n"
      "buffer_load_dwordx4 v41, s[24:27], s29 offen offset:0 lds\n"
      "buffer_load_dwordx4 v42, s[24:27], s29 offen offset:1024 lds\n"
      "buffer_load_dwordx4 v43, s[24:27], s29 offen offset:2048 lds\n"
      "buffer_load_dwordx4 v44, s[24:27], s29 offen offset:3072 lds\n"
      "s_lshl_b32 s29, s83, 7\n"
      "s_add_u32 s29, s29, s82\n"
      "s_lshl_b32 s80, s58, 11\n"
      "s_add_u32 s80, s80, 163840\n"
      "s_waitcnt vmcnt(12)\n"
      "s_barrier\n"
      "ds_read_b128 a[0:3], v45 offset:32768\n"
      "ds_read_b128 a[16:19], v47 offset:0\n"
      "ds_read_b128 a[20:23], v47 offset:2048\n"
      "ds_read_b128 a[24:27], v47 offset:4096\n"
      "ds_read_b128 a[28:31], v47 offset:6144\n"
      "ds_read_b128 a[32:35], v47 offset:8192\n"
      "ds_read_b128 a[36:39], v47 offset:10240\n"
      "ds_read_b128 a[40:43], v47 offset:12288\n"
      "ds_read_b128 a[44:47], v47 offset:14336\n"
      "ds_read_b128 a[4:7], v45 offset:34816\n"
      "ds_read_b128 a[8:11], v45 offset:36864\n"
      "ds_read_b128 a[12:15], v45 offset:38912\n"
      "s_waitcnt lgkmcnt(0)\n"
      "v_mfma_f32_16x16x32_bf16 v[64:67], a[0:3], a[16:19], 0\n"
      "ds_read_b128 a[48:51], v46 offset:32768\n"
      "s_add_u32 m0, s92, 131072\n"
      "v_mfma_f32_16x16x32_bf16 v[68:71], a[0:3], a[20:23], 0\n"
      "ds_read_b128 a[64:67], v48 offset:0\n"
      "buffer_load_dwordx4 v33, s[64:67], s28 offen offset:0 sc1 lds\n"
      "v_mfma_f32_16x16x32_bf16 v[72:75], a[0:3], a[24:27], 0\n"
      "v_mfma_f32_16x16x32_bf16 v[76:79], a[0:3], a[28:31], 0\n"
      "ds_read_b128 a[68:71], v48 offset:2048\n"
      "buffer_load_dwordx4 v34, s[64:67], s28 offen offset:1024 sc1 lds\n"
      "v_mfma_f32_16x16x32_bf16 v[80:83], a[0:3], a[32:35], 0\n"
      "v_mfma_f32_16x16x32_bf16 v[84:87], a[0:3], a[36:39], 0\n"
      "ds_read_b128 a[72:75], v48 offset:4096\n"
      "buffer_load_dwordx4 v35, s[64:67], s28 offen offset:2048 sc1 lds\n"
      "v_mfma_f32_16x16x32_bf16 v[88:91], a[0:3], a[40:43], 0\n"
      "ds_read_b128 a[76:79], v48 offset:6144\n"
      "v_mfma_f32_16x16x32_bf16 v[92:95], a[0:3], a[44:47], 0\n"
      "buffer_load_dwordx4 v36, s[64:67], s28 offen offset:3072 sc1 lds\n"
      "s_and_b32 s83, s10, 15\n"
      "s_lshr_b64 s[10:11], s[10:11], 4\n"
      "s_mul_i32 s28, s83, s30\n"
      "s_add_u32 s28, s28, s81\n"
      "v_mfma_f32_16x16x32_bf16 v[124:127], a[4:7], a[44:47], 0\n"
      "ds_read_b128 a[80:83], v48 offset:8192\n"
      "s_add_u32 m0, s85, 98304\n"
      "v_mfma_f32_16x16x32_bf16 v[120:123], a[4:7], a[40:43], 0\n"
      "buffer_load_dwordx4 v37, s[24:27], s29 offen offset:0 lds\n"
      "v_mfma_f32_16x16x32_bf16 v[116:119], a[4:7], a[36:39], 0\n"
      "ds_read_b128 a[84:87], v48 offset:10240\n"
      "v_mfma_f32_16x16x32_bf16 v[112:115], a[4:7], a[32:35], 0\n"
      "ds_read_b128 a[88:91], v48 offset:12288\n"
      "buffer_load_dwordx4 v38, s[24:27], s29 offen offset:1024 lds\n"
      "v_mfma_f32_16x16x32_bf16 v[108:111], a[4:7], a[28:31], 0\n"
      "v_mfma_f32_16x16x32_bf16 v[104:107], a[4:7], a[24:27], 0\n"
      "ds_read_b128 a[92:95], v48 offset:14336\n"
      "buffer_load_dwordx4 v39, s[24:27], s29 offen offset:2048 lds\n"
      "v_mfma_f32_16x16x32_bf16 v[100:103], a[4:7], a[20:23], 0\n"
      "v_mfma_f32_16x16x32_bf16 v[96:99], a[4:7], a[16:19], 0\n"
      "ds_read_b128 a[52:55], v46 offset:34816\n"
      "buffer_load_dwordx4 v40, s[24:27], s29 offen offset:3072 lds\n"
      "v_mfma_f32_16x16x32_bf16 v[128:131], a[8:11], a[16:19], 0\n"
      "ds_read_b128 a[56:59], v46 offset:36864\n"
      "s_add_u32 m0, s85, 102400\n"
      "v_mfma_f32_16x16x32_bf16 v[132:135], a[8:11], a[20:23], 0\n"
      "buffer_load_dwordx4 v41, s[24:27], s29 offen offset:0 lds\n"
      "v_mfma_f32_16x16x32_bf16 v[136:139], a[8:11], a[24:27], 0\n"
      "ds_read_b128 a[60:63], v46 offset:38912\n"
      "v_mfma_f32_16x16x32_bf16 v[140:143], a[8:11], a[28:31], 0\n"
      "buffer_load_dwordx4 v42, s[24:27], s29 offen offset:1024 lds\n"
      "v_mfma_f32_16x16x32_bf16 v[144:147], a[8:11], a[32:35], 0\n"
      "v_mfma_f32_16x16x32_bf16 v[148:151], a[8:11], a[36:39], 0\n"
      "buffer_load_dwordx4 v43, s[24:27], s29 offen offset:2048 lds\n"
      "v_mfma_f32_16x16x32_bf16 v[152:155], a[8:11], a[40:43], 0\n"
      "v_mfma_f32_16x16x32_bf16 v[156:159], a[8:11], a[44:47], 0\n"
      "buffer_load_dwordx4 v44, s[24:27], s29 offen offset:3072 lds\n"
      "s_lshl_b32 s29, s83, 7\n"
      "s_add_u32 s29, s29, s82\n"
      "v_mfma_f32_16x16x32_bf16 v[188:191], a[12:15], a[44:47], 0\n"
      "v_mfma_f32_16x16x32_bf16 v[184:187], a[12:15], a[40:43], 0\n"
      "s_add_u32 s96, s94, s85\n"
      "s_add_u32 s95, s94, s92\n"
      "v_mfma_f32_16x16x32_bf16 v[180:183], a[12:15], a[36:39], 0\n"
      "s_add_u32 s95, s95, 32768\n"
      "s_cmp_eq_u32 s94, 98304\n"
      "v_mfma_f32_16x16x32_bf16 v[176:179], a[12:15], a[32:35], 0\n"
      "s_cselect_b32 s97, 0xfffe8000, s99\n"
      "s_add_u32 s94, s94, s97\n"
      "v_mfma_f32_16x16x32_bf16 v[172:175], a[12:15], a[28:31], 0\n"
      "v_add_u32 v45, s97, v45\n"
      "v_add_u32 v46, s97, v46\n"
      "v_mfma_f32_16x16x32_bf16 v[168:171], a[12:15], a[24:27], 0\n"
      "v_add_u32 v47, s97, v47\n"
      "v_add_u32 v48, s97, v48\n"
      "v_mfma_f32_16x16x32_bf16 v[164:167], a[12:15], a[20:23], 0\n"
      "v_mfma_f32_16x16x32_bf16 v[160:163], a[12:15], a[16:19], 0\n"
      "s_mov_b32 s93, 12\n"
      "s_call_b64 s[34:35], Lk_nt\n"
      "s_waitcnt vmcnt(12) lgkmcnt(0)\n"
      "s_barrier\n"
      "v_mfma_f32_16x16x32_bf16 v[64:67], a[48:51], a[64:67], v[64:67]\n"
      "ds_read_b128 a[96:99], v45 offset:32768\n"
      "s_mov_b32 m0, s95\n"
      "v_mfma_f32_16x16x32_bf16 v[68:71], a[48:51], a[68:71], v[68:71]\n"
      "buffer_load_dwordx4 v33, s[64:67], s28 offen offset:0 sc1 lds\n"
      "v_mfma_f32_16x16x32_bf16 v[72:75], a[48:51], a[72:75], v[72:75]\n"
      "ds_read_b128 a[0:3], v47 offset:0\n"
      "v_mfma_f32_16x16x32_bf16 v[76:79], a[48:51], a[76:79], v[76:79]\n"
      "buffer_load_dwordx4 v34, s[64:67], s28 offen offset:1024 sc1 lds\n"
      "v_mfma_f32_16x16x32_bf16 v[80:83], a[48:51], a[80:83], v[80:83]\n"
      "ds_read_b128 a[4:7], v47 offset:2048\n"
      "v_mfma_f32_16x16x32_bf16 v[84:87], a[48:51], a[84:87], v[84:87]\n"
      "v_mfma_f32_16x16x32_bf16 v[88:91], a[48:51], a[88:91], v[88:91]\n"
      "ds_read_b128 a[8:11], v47 offset:4096\n"
      "buffer_load_dwordx4 v35, s[64:67], s28 offen offset:2048 sc1 lds\n"
      "v_mfma_f32_16x16x32_bf16 v[92:95], a[48:51], a[92:95], v[92:95]\n"
      "v_mfma_f32_16x16x32_bf16 v[124:127], a[52:55], a[92:95], v[124:127]\n"
      "ds_read_b128 a[12:15], v47 offset:6144\n"
      "buffer_load_dwordx4 v36, s[64:67], s28 offen offset:3072 sc1 lds\n"
      "v_mfma_f32_16x16x32_bf16 v[120:123], a[52:55], a[88:91], v[120:123]\n"
      "v_mfma_f32_16x16x32_bf16 v[116:119], a[52:55], a[84:87], v[116:119]\n"
      "ds_read_b128 a[100:103], v45 offset:34816\n"
      "s_add_u32 m0, s96, 0\n"
      "v_mfma_f32_16x16x32_bf16 v[112:115], a[52:55], a[80:83], v[112:115]\n"
      "buffer_load_dwordx4 v37, s[24:27], s29 offen offset:0 lds\n"
      "v_mfma_f32_16x16x32_bf16 v[108:111], a[52:55], a[76:79], v[108:111]\n"
      "ds_read_b128 a[104:107], v45 offset:36864\n"
      "v_mfma_f32_16x16x32_bf16 v[104:107], a[52:55], a[72:75], v[104:107]\n"
      "buffer_load_dwordx4 v38, s[24:27], s29 offen offset:1024 lds\n"
      "v_mfma_f32_16x16x32_bf16 v[100:103], a[52:55], a[68:71], v[100:103]\n"
      "ds_read_b128 a[108:111], v45 offset:38912\n"
      "v_mfma_f32_16x16x32_bf16 v[96:99], a[52:55], a[64:67], v[96:99]\n"
      "v_mfma_f32_16x16x32_bf16 v[128:131], a[56:59], a[64:67], v[128:131]\n"
      "ds_read_b128 a[32:35], v47 offset:8192\n"
      "buffer_load_dwordx4 v39, s[24:27], s29 offen offset:2048 lds\n"
      "v_mfma_f32_16x16x32_bf16 v[132:135], a[56:59], a[68:71], v[132:135]\n"
      "v_mfma_f32_16x16x32_bf16 v[136:139], a[56:59], a[72:75], v[136:139]\n"
      "ds_read_b128 a[36:39], v47 offset:10240\n"
      "buffer_load_dwordx4 v40, s[24:27], s29 offen offset:3072 lds\n"
      "v_mfma_f32_16x16x32_bf16 v[140:143], a[56:59], a[76:79], v[140:143]\n"
      "v_mfma_f32_16x16x32_bf16 v[144:147], a[56:59], a[80:83], v[144:147]\n"
      "ds_read_b128 a[40:43], v47 offset:12288\n"
      "s_add_u32 m0, s96, 4096\n"
      "v_mfma_f32_16x16x32_bf16 v[148:151], a[56:59], a[84:87], v[148:151]\n"
      "buffer_load_dwordx4 v41, s[24:27], s29 offen offset:0 lds\n"
      "v_mfma_f32_16x16x32_bf16 v[152:155], a[56:59], a[88:91], v[152:155]\n"
      "ds_read_b128 a[44:47], v47 offset:14336\n"
      "v_mfma_f32_16x16x32_bf16 v[156:159], a[56:59], a[92:95], v[156:159]\n"
      "buffer_load_dwordx4 v42, s[24:27], s29 offen offset:1024 lds\n"
      "v_mfma_f32_16x16x32_bf16 v[188:191], a[60:63], a[92:95], v[188:191]\n"
      "v_mfma_f32_16x16x32_bf16 v[184:187], a[60:63], a[88:91], v[184:187]\n"
      "v_mfma_f32_16x16x32_bf16 v[180:183], a[60:63], a[84:87], v[180:183]\n"
      "buffer_load_dwordx4 v43, s[24:27], s29 offen offset:2048 lds\n"
      "v_mfma_f32_16x16x32_bf16 v[176:179], a[60:63], a[80:83], v[176:179]\n"
      "v_mfma_f32_16x16x32_bf16 v[172:175], a[60:63], a[76:79], v[172:175]\n"
      "buffer_load_dwordx4 v44, s[24:27], s29 offen offset:3072 lds\n"
      "v_mfma_f32_16x16x32_bf16 v[168:171], a[60:63], a[72:75], v[168:171]\n"
      "v_mfma_f32_16x16x32_bf16 v[164:167], a[60:63], a[68:71], v[164:167]\n"
      "v_mfma_f32_16x16x32_bf16 v[160:163], a[60:63], a[64:67], v[160:163]\n"
      "s_waitcnt lgkmcnt(0)\n"
      "v_mfma_f32_16x16x32_bf16 v[64:67], a[96:99], a[0:3], v[64:67]\n"
      "ds_read_b128 a[112:115], v46 offset:32768\n"
      "s_movk_i32 s30, 16384\n"
      "s_mov_b32 s26, 0x800000\n"
      "v_mfma_f32_16x16x32_bf16 v[96:99], a[100:103], a[0:3], v[96:99]\n"
      "ds_read_b128 a[16:19], v48 offset:0\n"
      "s_mov_b32 s24, s38\n"
      "s_and_b32 s25, s39, 0xffff\n"
      "v_mfma_f32_16x16x32_bf16 v[128:131], a[104:107], a[0:3], v[128:131]\n"
      "ds_read_b128 a[20:23], v48 offset:2048\n"
      "s_mov_b32 s81, s76\n"
      "s_mov_b32 s82, s77\n"
      "v_mfma_f32_16x16x32_bf16 v[160:163], a[108:111], a[0:3], v[160:163]\n"
      "ds_read_b128 a[24:27], v48 offset:4096\n"
      "s_lshr_b64 s[10:11], s[14:15], 0\n"
      "s_and_b32 s83, s10, 15\n"
      "v_mfma_f32_16x16x32_bf16 v[164:167], a[108:111], a[4:7], v[164:167]\n"
      "ds_read_b128 a[28:31], v48 offset:6144\n"
      "s_lshr_b64 s[10:11], s[10:11], 4\n"
      "s_mul_i32 s28, s83, s30\n"
      "v_mfma_f32_16x16x32_bf16 v[132:135], a[104:107], a[4:7], v[132:135]\n"
      "ds_read_b128 a[116:119], v46 offset:34816\n"
      "s_add_u32 s28, s28, s81\n"
      "s_lshl_b32 s29, s83, 7\n"
      "v_mfma_f32_16x16x32_bf16 v[100:103], a[100:103], a[4:7], v[100:103]\n"
      "ds_read_b128 a[120:123], v46 offset:36864\n"
      "s_add_u32 s29, s29, s82\n"
      "v_accvgpr_read_b32 v33, a220\n"
      "v_mfma_f32_16x16x32_bf16 v[68:71], a[96:99], a[4:7], v[68:71]\n"
      "ds_read_b128 a[124:127], v46 offset:38912\n"
      "v_accvgpr_read_b32 v34, a220\n"
      "v_accvgpr_read_b32 v35, a220\n"
      "v_mfma_f32_16x16x32_bf16 v[72:75], a[96:99], a[8:11], v[72:75]\n"
      "ds_read_b128 a[48:51], v48 offset:8192\n"
      "v_accvgpr_read_b32 v36, a220\n"
      "v_accvgpr_read_b32 v37, a208\n"
      "v_mfma_f32_16x16x32_bf16 v[104:107], a[100:103], a[8:11], v[104:107]\n"
      "ds_read_b128 a[52:55], v48 offset:10240\n"
      "v_accvgpr_read_b32 v38, a209\n"
      "v_accvgpr_read_b32 v39, a210\n"
      "v_mfma_f32_16x16x32_bf16 v[136:139], a[104:107], a[8:11], v[136:139]\n"
      "ds_read_b128 a[56:59], v48 offset:12288\n"
      "v_accvgpr_read_b32 v40, a211\n"
      "v_accvgpr_read_b32 v41, a212\n"
      "v_mfma_f32_16x16x32_bf16 v[168:171], a[108:111], a[8:11], v[168:171]\n"
      "ds_read_b128 a[60:63], v48 offset:14336\n"
      "v_accvgpr_read_b32 v42, a213\n"
      "v_accvgpr_read_b32 v43, a214\n"
      "v_mfma_f32_16x16x32_bf16 v[172:175], a[108:111], a[12:15], v[172:175]\n"
      "s_cmp_eq_u32 s94, 98304\n"
      "s_cselect_b32 s97, 0xfffe8000, s99\n"
      "s_add_u32 s94, s94, s97\n"
      "v_mfma_f32_16x16x32_bf16 v[140:143], a[104:107], a[12:15], v[140:143]\n"
      "v_add_u32 v45, s97, v45\n"
      "v_add_u32 v46, s97, v46\n"
      "v_accvgpr_read_b32 v44, a215\n"
      "v_mfma_f32_16x16x32_bf16 v[108:111], a[100:103], a[12:15], v[108:111]\n"
      "v_add_u32 v47, s97, v47\n"
      "v_add_u32 v48, s97, v48\n"
      "v_mfma_f32_16x16x32_bf16 v[76:79], a[96:99], a[12:15], v[76:79]\n"
      "s_waitcnt vmcnt(12) lgkmcnt(0)\n"
      "s_barrier\n"
      "v_mfma_f32_16x16x32_bf16 v[64:67], a[112:115], a[16:19], v[64:67]\n"
      "ds_read_b128 a[128:131], v45 offset:32768\n"
      "s_add_u32 m0, s85, 49152\n"
      "v_mfma_f32_16x16x32_bf16 v[96:99], a[116:119], a[16:19], v[96:99]\n"
      "ds_read_b128 a[0:3], v47 offset:0\n"
      "buffer_load_dwordx4 v37, s[24:27], s29 offen offset:0 lds\n"
      "v_mfma_f32_16x16x32_bf16 v[128:131], a[120:123], a[16:19], v[128:131]\n"
      "ds_read_b128 a[4:7], v47 offset:2048\n"
      "v_mfma_f32_16x16x32_bf16 v[160:163], a[124:127], a[16:19], v[160:163]\n"
      "ds_read_b128 a[8:11], v47 offset:4096\n"
      "v_mfma_f32_16x16x32_bf16 v[164:167], a[124:127], a[20:23], v[164:167]\n"
      "ds_read_b128 a[12:15], v47 offset:6144\n"
      "buffer_load_dwordx4 v38, s[24:27], s29 offen offset:1024 lds\n"
      "v_mfma_f32_16x16x32_bf16 v[132:135], a[120:123], a[20:23], v[132:135]\n"
      "ds_read_b128 a[132:135], v45 offset:34816\n"
      "v_mfma_f32_16x16x32_bf16 v[100:103], a[116:119], a[20:23], v[100:103]\n"
      "ds_read_b128 a[136:139], v45 offset:36864\n"
      "v_mfma_f32_16x16x32_bf16 v[68:71], a[112:115], a[20:23], v[68:71]\n"
      "ds_read_b128 a[140:143], v45 offset:38912\n"
      "buffer_load_dwordx4 v39, s[24:27], s29 offen offset:2048 lds\n"
      "v_mfma_f32_16x16x32_bf16 v[72:75], a[112:115], a[24:27], v[72:75]\n"
      "ds_read_b128 a[64:67], v47 offset:8192\n"
      "v_mfma_f32_16x16x32_bf16 v[104:107], a[116:119], a[24:27], v[104:107]\n"
      "ds_read_b128 a[68:71], v47 offset:10240\n"
      "v_mfma_f32_16x16x32_bf16 v[136:139], a[120:123], a[24:27], v[136:139]\n"
      "ds_read_b128 a[72:75], v47 offset:12288\n"
      "buffer_load_dwordx4 v40, s[24:27], s29 offen offset:3072 lds\n"
      "v_mfma_f32_16x16x32_bf16 v[168:171], a[124:127], a[24:27], v[168:171]\n"
      "ds_read_b128 a[76:79], v47 offset:14336\n"
      "v_mfma_f32_16x16x32_bf16 v[172:175], a[124:127], a[28:31], v[172:175]\n"
      "v_mfma_f32_16x16x32_bf16 v[140:143], a[120:123], a[28:31], v[140:143]\n"
      "v_mfma_f32_16x16x32_bf16 v[108:111], a[116:119], a[28:31], v[108:111]\n"
      "v_mfma_f32_16x16x32_bf16 v[76:79], a[112:115], a[28:31], v[76:79]\n"
      "s_waitcnt lgkmcnt(4)\n"
      "v_mfma_f32_16x16x32_bf16 v[64:67], a[128:131], a[0:3], v[64:67]\n"
      "ds_read_b128 a[144:147], v46 offset:32768\n"
      "s_add_u32 m0, s85, 53248\n"
      "v_mfma_f32_16x16x32_bf16 v[96:99], a[132:135], a[0:3], v[96:99]\n"
      "ds_read_b128 a[16:19], v48 offset:0\n"
      "buffer_load_dwordx4 v41, s[24:27], s29 offen offset:0 lds\n"
      "v_mfma_f32_16x16x32_bf16 v[128:131], a[136:139], a[0:3], v[128:131]\n"
      "ds_read_b128 a[20:23], v48 offset:2048\n"
      "v_mfma_f32_16x16x32_bf16 v[160:163], a[140:143], a[0:3], v[160:163]\n"
      "ds_read_b128 a[24:27], v48 offset:4096\n"
      "v_mfma_f32_16x16x32_bf16 v[164:167], a[140:143], a[4:7], v[164:167]\n"
      "ds_read_b128 a[28:31], v48 offset:6144\n"
      "buffer_load_dwordx4 v42, s[24:27], s29 offen offset:1024 lds\n"
      "v_mfma_f32_16x16x32_bf16 v[132:135], a[136:139], a[4:7], v[132:135]\n"
      "ds_read_b128 a[148:151], v46 offset:34816\n"
      "v_mfma_f32_16x16x32_bf16 v[100:103], a[132:135], a[4:7], v[100:103]\n"
      "ds_read_b128 a[152:155], v46 offset:36864\n"
      "v_mfma_f32_16x16x32_bf16 v[68:71], a[128:131], a[4:7], v[68:71]\n"
      "ds_read_b128 a[156:159], v46 offset:38912\n"
      "buffer_load_dwordx4 v43, s[24:27], s29 offen offset:2048 lds\n"
      "v_mfma_f32_16x16x32_bf16 v[72:75], a[128:131], a[8:11], v[72:75]\n"
      "ds_read_b128 a[80:83], v48 offset:8192\n"
      "v_mfma_f32_16x16x32_bf16 v[104:107], a[132:135], a[8:11], v[104:107]\n"
      "ds_read_b128 a[84:87], v48 offset:10240\n"
      "v_mfma_f32_16x16x32_bf16 v[136:139], a[136:139], a[8:11], v[136:139]\n"
      "ds_read_b128 a[88:91], v48 offset:12288\n"
      "buffer_load_dwordx4 v44, s[24:27], s29 offen offset:3072 lds\n"
      "v_mfma_f32_16x16x32_bf16 v[168:171], a[140:143], a[8:11], v[168:171]\n"
      "ds_read_b128 a[92:95], v48 offset:14336\n"
      "s_and_b32 s83, s10, 15\n"
      "s_lshr_b64 s[10:11], s[10:11], 4\n"
      "v_mfma_f32_16x16x32_bf16 v[172:175], a[140:143], a[12:15], v[172:175]\n"
      "s_lshl_b32 s29, s83, 7\n"
      "s_add_u32 s29, s29, s82\n"
      "s_cmp_eq_u32 s94, 98304\n"
      "s_cselect_b32 s97, 0xfffe8000, s99\n"
      "s_add_u32 s94, s94, s97\n"
      "v_mfma_f32_16x16x32_bf16 v[140:143], a[136:139], a[12:15], v[140:143]\n"
      "v_add_u32 v45, s97, v45\n"
      "v_add_u32 v46, s97, v46\n"
      "v_mfma_f32_16x16x32_bf16 v[108:111], a[132:135], a[12:15], v[108:111]\n"
      "v_add_u32 v47, s97, v47\n"
      "v_add_u32 v48, s97, v48\n"
      "v_mfma_f32_16x16x32_bf16 v[76:79], a[128:131], a[12:15], v[76:79]\n"
      "s_waitcnt vmcnt(8) lgkmcnt(0)\n"
      "s_barrier\n"
      "v_mfma_f32_16x16x32_bf16 v[64:67], a[144:147], a[16:19], v[64:67]\n"
      "ds_read_b128 a[160:163], v45 offset:32768\n"
      "v_mfma_f32_16x16x32_bf16 v[96:99], a[148:151], a[16:19], v[96:99]\n"
      "ds_read_b128 a[0:3], v47 offset:0\n"
      "v_mfma_f32_16x16x32_bf16 v[128:131], a[152:155], a[16:19], v[128:131]\n"
      "ds_read_b128 a[4:7], v47 offset:2048\n"
      "v_mfma_f32_16x16x32_bf16 v[160:163], a[156:159], a[16:19], v[160:163]\n"
      "ds_read_b128 a[8:11], v47 offset:4096\n"
      "v_mfma_f32_16x16x32_bf16 v[164:167], a[156:159], a[20:23], v[164:167]\n"
      "ds_read_b128 a[12:15], v47 offset:6144\n"
      "v_mfma_f32_16x16x32_bf16 v[132:135], a[152:155], a[20:23], v[132:135]\n"
      "ds_read_b128 a[164:167], v45 offset:34816\n"
      "v_mfma_f32_16x16x32_bf16 v[100:103], a[148:151], a[20:23], v[100:103]\n"
      "ds_read_b128 a[168:171], v45 offset:36864\n"
      "v_mfma_f32_16x16x32_bf16 v[68:71], a[144:147], a[20:23], v[68:71]\n"
      "ds_read_b128 a[172:175], v45 offset:38912\n"
      "v_mfma_f32_16x16x32_bf16 v[72:75], a[144:147], a[24:27], v[72:75]\n"
      "ds_read_b128 a[192:195], v47 offset:8192\n"
      "v_mfma_f32_16x16x32_bf16 v[104:107], a[148:151], a[24:27], v[104:107]\n"
      "ds_read_b128 a[196:199], v47 offset:10240\n"
      "v_mfma_f32_16x16x32_bf16 v[136:139], a[152:155], a[24:27], v[136:139]\n"
      "ds_read_b128 a[200:203], v47 offset:12288\n"
      "v_mfma_f32_16x16x32_bf16 v[168:171], a[156:159], a[24:27], v[168:171]\n"
      "ds_read_b128 a[204:207], v47 offset:14336\n"
      "v_mfma_f32_16x16x32_bf16 v[172:175], a[156:159], a[28:31], v[172:175]\n"
      "v_mfma_f32_16x16x32_bf16 v[140:143], a[152:155], a[28:31], v[140:143]\n"
      "v_mfma_f32_16x16x32_bf16 v[108:111], a[148:151], a[28:31], v[108:111]\n"
      "v_mfma_f32_16x16x32_bf16 v[76:79], a[144:147], a[28:31], v[76:79]\n"
      "s_waitcnt lgkmcnt(4)\n"
      "v_mfma_f32_16x16x32_bf16 v[64:67], a[160:163], a[0:3], v[64:67]\n"
      "ds_read_b128 a[176:179], v46 offset:32768\n"
      "v_mfma_f32_16x16x32_bf16 v[96:99], a[164:167], a[0:3], v[96:99]\n"
      "ds_read_b128 a[16:19], v48 offset:0\n"
      "v_mfma_f32_16x16x32_bf16 v[128:131], a[168:171], a[0:3], v[128:131]\n"
      "v_mfma_f32_16x16x32_bf16 v[160:163], a[172:175], a[0:3], v[160:163]\n"
      "ds_read_b128 a[20:23], v48 offset:2048\n"
      "v_mfma_f32_16x16x32_bf16 v[164:167], a[172:175], a[4:7], v[164:167]\n"
      "ds_read_b128 a[24:27], v48 offset:4096\n"
      "v_mfma_f32_16x16x32_bf16 v[132:135], a[168:171], a[4:7], v[132:135]\n"
      "v_mfma_f32_16x16x32_bf16 v[100:103], a[164:167], a[4:7], v[100:103]\n"
      "ds_read_b128 a[28:31], v48 offset:6144\n"
      "v_mfma_f32_16x16x32_bf16 v[68:71], a[160:163], a[4:7], v[68:71]\n"
      "ds_read_b128 a[180:183], v46 offset:34816\n"
      "v_mfma_f32_16x16x32_bf16 v[72:75], a[160:163], a[8:11], v[72:75]\n"
      "v_mfma_f32_16x16x32_bf16 v[104:107], a[164:167], a[8:11], v[104:107]\n"
      "ds_read_b128 a[184:187], v46 offset:36864\n"
      "v_mfma_f32_16x16x32_bf16 v[136:139], a[168:171], a[8:11], v[136:139]\n"
      "ds_read_b128 a[188:191], v46 offset:38912\n"
      "v_mfma_f32_16x16x32_bf16 v[168:171], a[172:175], a[8:11], v[168:171]\n"
      "v_mfma_f32_16x16x32_bf16 v[172:175], a[172:175], a[12:15], v[172:175]\n"
      "v_mfma_f32_16x16x32_bf16 v[140:143], a[168:171], a[12:15], v[140:143]\n"
      "v_mfma_f32_16x16x32_bf16 v[108:111], a[164:167], a[12:15], v[108:111]\n"
      "v_mfma_f32_16x16x32_bf16 v[76:79], a[160:163], a[12:15], v[76:79]\n"
      "s_waitcnt lgkmcnt(0)\n"
      "v_mfma_f32_16x16x32_bf16 v[64:67], a[176:179], a[16:19], v[64:67]\n"
      "ds_read_b128 a[0:3], v48 offset:8192\n"
      "v_mfma_f32_16x16x32_bf16 v[96:99], a[180:183], a[16:19], v[96:99]\n"
      "ds_read_b128 a[4:7], v48 offset:10240\n"
      "v_mfma_f32_16x16x32_bf16 v[128:131], a[184:187], a[16:19], v[128:131]\n"
      "ds_read_b128 a[8:11], v48 offset:12288\n"
      "v_mfma_f32_16x16x32_bf16 v[160:163], a[188:191], a[16:19], v[160:163]\n"
      "ds_read_b128 a[12:15], v48 offset:14336\n"
      "v_mfma_f32_16x16x32_bf16 v[164:167], a[188:191], a[20:23], v[164:167]\n"
      "v_mfma_f32_16x16x32_bf16 v[132:135], a[184:187], a[20:23], v[132:135]\n"
      "v_mfma_f32_16x16x32_bf16 v[100:103], a[180:183], a[20:23], v[100:103]\n"
      "v_mfma_f32_16x16x32_bf16 v[68:71], a[176:179], a[20:23], v[68:71]\n"
      "v_mfma_f32_16x16x32_bf16 v[72:75], a[176:179], a[24:27], v[72:75]\n"
      "v_mfma_f32_16x16x32_bf16 v[104:107], a[180:183], a[24:27], v[104:107]\n"
      "v_mfma_f32_16x16x32_bf16 v[136:139], a[184:187], a[24:27], v[136:139]\n"
      "v_mfma_f32_16x16x32_bf16 v[168:171], a[188:191], a[24:27], v[168:171]\n"
      "v_mfma_f32_16x16x32_bf16 v[172:175], a[188:191], a[28:31], v[172:175]\n"
      "v_mfma_f32_16x16x32_bf16 v[140:143], a[184:187], a[28:31], v[140:143]\n"
      "v_mfma_f32_16x16x32_bf16 v[108:111], a[180:183], a[28:31], v[108:111]\n"
      "v_mfma_f32_16x16x32_bf16 v[76:79], a[176:179], a[28:31], v[76:79]\n"
      "s_waitcnt lgkmcnt(0)\n"
      "s_barrier\n"
      "v_mfma_f32_16x16x32_bf16 v[80:83], a[96:99], a[32:35], v[80:83]\n"
      "v_cvt_pk_bf16_f32 v64, v64, v65\n"
      "v_cvt_pk_bf16_f32 v65, v66, v67\n"
      "v_cvt_pk_bf16_f32 v66, v96, v97\n"
      "v_mfma_f32_16x16x32_bf16 v[112:115], a[100:103], a[32:35], v[112:115]\n"
      "v_cvt_pk_bf16_f32 v67, v98, v99\n"
      "s_nop 0\n"
      "v_permlane16_swap_b32 v64, v66\n"
      "v_mfma_f32_16x16x32_bf16 v[144:147], a[104:107], a[32:35], v[144:147]\n"
      "v_permlane16_swap_b32 v65, v67\n"
      "buffer_store_dwordx4 v[64:67], v58, s[64:67], s72 offen offset:0 sc1\n"
      "v_cvt_pk_bf16_f32 v128, v128, v129\n"
      "v_mfma_f32_16x16x32_bf16 v[176:179], a[108:111], a[32:35], v[176:179]\n"
      "v_cvt_pk_bf16_f32 v129, v130, v131\n"
      "v_cvt_pk_bf16_f32 v130, v160, v161\n"
      "v_cvt_pk_bf16_f32 v131, v162, v163\n"
      "v_mfma_f32_16x16x32_bf16 v[180:183], a[108:111], a[36:39], v[180:183]\n"
      "s_nop 0\n"
      "v_permlane16_swap_b32 v128, v130\n"
      "v_permlane16_swap_b32 v129, v131\n"
      "v_mfma_f32_16x16x32_bf16 v[148:151], a[104:107], a[36:39], v[148:151]\n"
      "buffer_store_dwordx4 v[128:131], v58, s[64:67], s72 offen offset:1024 sc1\n"
      "v_cvt_pk_bf16_f32 v68, v68, v69\n"
      "v_cvt_pk_bf16_f32 v69, v70, v71\n"
      "v_mfma_f32_16x16x32_bf16 v[116:119], a[100:103], a[36:39], v[116:119]\n"
      "v_cvt_pk_bf16_f32 v70, v100, v101\n"
      "v_cvt_pk_bf16_f32 v71, v102, v103\n"
      "s_nop 0\n"
      "v_mfma_f32_16x16x32_bf16 v[84:87], a[96:99], a[36:39], v[84:87]\n"
      "v_permlane16_swap_b32 v68, v70\n"
      "v_permlane16_swap_b32 v69, v71\n"
      "buffer_store_dwordx4 v[68:71], v58, s[64:67], s72 offen offset:2048 sc1\n"
      "v_mfma_f32_16x16x32_bf16 v[88:91], a[96:99], a[40:43], v[88:91]\n"
      "v_cvt_pk_bf16_f32 v132, v132, v133\n"
      "v_cvt_pk_bf16_f32 v133, v134, v135\n"
      "v_cvt_pk_bf16_f32 v134, v164, v165\n"
      "v_mfma_f32_16x16x32_bf16 v[120:123], a[100:103], a[40:43], v[120:123]\n"
      "v_cvt_pk_bf16_f32 v135, v166, v167\n"
      "s_nop 0\n"
      "v_permlane16_swap_b32 v132, v134\n"
      "v_mfma_f32_16x16x32_bf16 v[152:155], a[104:107], a[40:43], v[152:155]\n"
      "v_permlane16_swap_b32 v133, v135\n"
      "buffer_store_dwordx4 v[132:135], v58, s[64:67], s72 offen offset:3072 sc1\n"
      "v_cvt_pk_bf16_f32 v72, v72, v73\n"
      "v_mfma_f32_16x16x32_bf16 v[184:187], a[108:111], a[40:43], v[184:187]\n"
      "v_cvt_pk_bf16_f32 v73, v74, v75\n"
      "v_cvt_pk_bf16_f32 v74, v104, v105\n"
      "v_cvt_pk_bf16_f32 v75, v106, v107\n"
      "v_mfma_f32_16x16x32_bf16 v[188:191], a[108:111], a[44:47], v[188:191]\n"
      "s_nop 0\n"
      "v_permlane16_swap_b32 v72, v74\n"
      "v_permlane16_swap_b32 v73, v75\n"
      "v_mfma_f32_16x16x32_bf16 v[156:159], a[104:107], a[44:47], v[156:159]\n"
      "buffer_store_dwordx4 v[72:75], v58, s[64:67], s73 offen offset:0 sc1\n"
      "v_cvt_pk_bf16_f32 v136, v136, v137\n"
      "v_cvt_pk_bf16_f32 v137, v138, v139\n"
      "v_mfma_f32_16x16x32_bf16 v[124:127], a[100:103], a[44:47], v[124:127]\n"
      "v_cvt_pk_bf16_f32 v138, v168, v169\n"
      "v_cvt_pk_bf16_f32 v139, v170, v171\n"
      "s_nop 0\n"
      "v_mfma_f32_16x16x32_bf16 v[92:95], a[96:99], a[44:47], v[92:95]\n"
      "v_permlane16_swap_b32 v136, v138\n"
      "v_permlane16_swap_b32 v137, v139\n"
      "buffer_store_dwordx4 v[136:139], v58, s[64:67], s73 offen offset:1024 sc1\n"
      "v_mfma_f32_16x16x32_bf16 v[80:83], a[112:115], a[48:51], v[80:83]\n"
      "v_cvt_pk_bf16_f32 v76, v76, v77\n"
      "v_cvt_pk_bf16_f32 v77, v78, v79\n"
      "v_cvt_pk_bf16_f32 v78, v108, v109\n"
      "v_mfma_f32_16x16x32_bf16 v[112:115], a[116:119], a[48:51], v[112:115]\n"
      "v_cvt_pk_bf16_f32 v79, v110, v111\n"
      "s_nop 0\n"
      "v_permlane16_swap_b32 v76, v78\n"
      "v_mfma_f32_16x16x32_bf16 v[144:147], a[120:123], a[48:51], v[144:147]\n"
      "v_permlane16_swap_b32 v77, v79\n"
      "buffer_store_dwordx4 v[76:79], v58, s[64:67], s73 offen offset:2048 sc1\n"
      "v_cvt_pk_bf16_f32 v140, v140, v141\n"
      "v_mfma_f32_16x16x32_bf16 v[176:179], a[124:127], a[48:51], v[176:179]\n"
      "v_cvt_pk_bf16_f32 v141, v142, v143\n"
      "v_cvt_pk_bf16_f32 v142, v172, v173\n"
      "v_cvt_pk_bf16_f32 v143, v174, v175\n"
      "v_mfma_f32_16x16x32_bf16 v[180:183], a[124:127], a[52:55], v[180:183]\n"
      "s_nop 0\n"
      "v_permlane16_swap_b32 v140, v142\n"
      "v_permlane16_swap_b32 v141, v143\n"
      "v_mfma_f32_16x16x32_bf16 v[148:151], a[120:123], a[52:55], v[148:151]\n"
      "buffer_store_dwordx4 v[140:143], v58, s[64:67], s73 offen offset:3072 sc1\n"
      "v_accvgpr_read_b32 v45, a223\n"
      "v_accvgpr_read_b32 v46, a224\n"
      "v_mfma_f32_16x16x32_bf16 v[116:119], a[116:119], a[52:55], v[116:119]\n"
      "v_accvgpr_read_b32 v47, a225\n"
      "v_accvgpr_read_b32 v48, a226\n"
      "v_add_u32 v45, 49152, v45\n"
      "v_mfma_f32_16x16x32_bf16 v[84:87], a[112:115], a[52:55], v[84:87]\n"
      "v_add_u32 v46, 49152, v46\n"
      "v_add_u32 v47, 49152, v47\n"
      "v_add_u32 v48, 49152, v48\n"
      "v_mfma_f32_16x16x32_bf16 v[88:91], a[112:115], a[56:59], v[88:91]\n"
      "s_mov_b32 s94, 49152\n"
      "s_lshl_b32 s76, s5, 7\n"
      "s_add_u32 s76, s76, s80\n"
      "v_mfma_f32_16x16x32_bf16 v[120:123], a[116:119], a[56:59], v[120:123]\n"
      "v_mfma_f32_16x16x32_bf16 v[152:155], a[120:123], a[56:59], v[152:155]\n"
      "v_mfma_f32_16x16x32_bf16 v[184:187], a[124:127], a[56:59], v[184:187]\n"
      "v_mfma_f32_16x16x32_bf16 v[188:191], a[124:127], a[60:63], v[188:191]\n"
      "v_mfma_f32_16x16x32_bf16 v[156:159], a[120:123], a[60:63], v[156:159]\n"
      "v_mfma_f32_16x16x32_bf16 v[124:127], a[116:119], a[60:63], v[124:127]\n"
      "v_mfma_f32_16x16x32_bf16 v[92:95], a[112:115], a[60:63], v[92:95]\n"
      "v_mfma_f32_16x16x32_bf16 v[80:83], a[128:131], a[64:67], v[80:83]\n"
      "v_mfma_f32_16x16x32_bf16 v[112:115], a[132:135], a[64:67], v[112:115]\n"
      "v_mfma_f32_16x16x32_bf16 v[144:147], a[136:139], a[64:67], v[144:147]\n"
      "v_mfma_f32_16x16x32_bf16 v[176:179], a[140:143], a[64:67], v[176:179]\n"
      "v_mfma_f32_16x16x32_bf16 v[180:183], a[140:143], a[68:71], v[180:183]\n"
      "v_mfma_f32_16x16x32_bf16 v[148:151], a[136:139], a[68:71], v[148:151]\n"
      "v_mfma_f32_16x16x32_bf16 v[116:119], a[132:135], a[68:71], v[116:119]\n"
      "v_mfma_f32_16x16x32_bf16 v[84:87], a[128:131], a[68:71], v[84:87]\n"
      "v_mfma_f32_16x16x32_bf16 v[88:91], a[128:131], a[72:75], v[88:91]\n"
      "v_mfma_f32_16x16x32_bf16 v[120:123], a[132:135], a[72:75], v[120:123]\n"
      "v_mfma_f32_16x16x32_bf16 v[152:155], a[136:139], a[72:75], v[152:155]\n"
      "v_mfma_f32_16x16x32_bf16 v[184:187], a[140:143], a[72:75], v[184:187]\n"
      "v_mfma_f32_16x16x32_bf16 v[188:191], a[140:143], a[76:79], v[188:191]\n"
      "v_mfma_f32_16x16x32_bf16 v[156:159], a[136:139], a[76:79], v[156:159]\n"
      "v_mfma_f32_16x16x32_bf16 v[124:127], a[132:135], a[76:79], v[124:127]\n"
      "v_mfma_f32_16x16x32_bf16 v[92:95], a[128:131], a[76:79], v[92:95]\n"
      "v_mfma_f32_16x16x32_bf16 v[80:83], a[144:147], a[80:83], v[80:83]\n"
      "v_mfma_f32_16x16x32_bf16 v[112:115], a[148:151], a[80:83], v[112:115]\n"
      "s_add_u32 m0, s85, 98304\n"
      "v_mfma_f32_16x16x32_bf16 v[144:147], a[152:155], a[80:83], v[144:147]\n"
      "buffer_load_dwordx4 v37, s[24:27], s29 offen offset:0 lds\n"
      "v_mfma_f32_16x16x32_bf16 v[176:179], a[156:159], a[80:83], v[176:179]\n"
      "v_mfma_f32_16x16x32_bf16 v[180:183], a[156:159], a[84:87], v[180:183]\n"
      "buffer_load_dwordx4 v38, s[24:27], s29 offen offset:1024 lds\n"
      "v_mfma_f32_16x16x32_bf16 v[148:151], a[152:155], a[84:87], v[148:151]\n"
      "v_mfma_f32_16x16x32_bf16 v[116:119], a[148:151], a[84:87], v[116:119]\n"
      "buffer_load_dwordx4 v39, s[24:27], s29 offen offset:2048 lds\n"
      "v_mfma_f32_16x16x32_bf16 v[84:87], a[144:147], a[84:87], v[84:87]\n"
      "v_mfma_f32_16x16x32_bf16 v[88:91], a[144:147], a[88:91], v[88:91]\n"
      "v_mfma_f32_16x16x32_bf16 v[120:123], a[148:151], a[88:91], v[120:123]\n"
      "buffer_load_dwordx4 v40, s[24:27], s29 offen offset:3072 lds\n"
      "v_mfma_f32_16x16x32_bf16 v[152:155], a[152:155], a[88:91], v[152:155]\n"
      "s_add_u32 m0, s85, 102400\n"
      "v_mfma_f32_16x16x32_bf16 v[184:187], a[156:159], a[88:91], v[184:187]\n"
      "buffer_load_dwordx4 v41, s[24:27], s29 offen offset:0 lds\n"
      "v_mfma_f32_16x16x32_bf16 v[188:191], a[156:159], a[92:95], v[188:191]\n"
      "v_mfma_f32_16x16x32_bf16 v[156:159], a[152:155], a[92:95], v[156:159]\n"
      "buffer_load_dwordx4 v42, s[24:27], s29 offen offset:1024 lds\n"
      "v_mfma_f32_16x16x32_bf16 v[124:127], a[148:151], a[92:95], v[124:127]\n"
      "v_mfma_f32_16x16x32_bf16 v[92:95], a[144:147], a[92:95], v[92:95]\n"
      "v_mfma_f32_16x16x32_bf16 v[80:83], a[160:163], a[192:195], v[80:83]\n"
      "buffer_load_dwordx4 v43, s[24:27], s29 offen offset:2048 lds\n"
      "v_mfma_f32_16x16x32_bf16 v[112:115], a[164:167], a[192:195], v[112:115]\n"
      "v_mfma_f32_16x16x32_bf16 v[144:147], a[168:171], a[192:195], v[144:147]\n"
      "buffer_load_dwordx4 v44, s[24:27], s29 offen offset:3072 lds\n"
      "s_and_b32 s83, s10, 15\n"
      "s_lshr_b64 s[10:11], s[10:11], 4\n"
      "s_lshl_b32 s29, s83, 7\n"
      "s_add_u32 s29, s29, s82\n"
      "v_mfma_f32_16x16x32_bf16 v[176:179], a[172:175], a[192:195], v[176:179]\n"
      "v_mfma_f32_16x16x32_bf16 v[180:183], a[172:175], a[196:199], v[180:183]\n"
      "v_mfma_f32_16x16x32_bf16 v[148:151], a[168:171], a[196:199], v[148:151]\n"
      "v_mfma_f32_16x16x32_bf16 v[116:119], a[164:167], a[196:199], v[116:119]\n"
      "v_mfma_f32_16x16x32_bf16 v[84:87], a[160:163], a[196:199], v[84:87]\n"
      "s_waitcnt vmcnt(8)\n"
      "s_barrier\n"
      "s_cmp_eq_u32 s61, 0\n"
      "s_cbranch_scc0 Lk_pub_13\n"
      "v_mov_b32 v6, s54\n"
      "v_mov_b32 v7, s55\n"
      "v_min_u32 v5, 1, v4\n"
      "v_lshlrev_b32 v5, 30, v5\n"
      "buffer_store_dwordx2 v[6:7], v5, s[64:67], s76 offen sc1\n"
      "Lk_pub_13:\n"
      "v_mfma_f32_16x16x32_bf16 v[88:91], a[160:163], a[200:203], v[88:91]\n"
      "s_add_u32 m0, s85, 0\n"
      "v_mfma_f32_16x16x32_bf16 v[120:123], a[164:167], a[200:203], v[120:123]\n"
      "buffer_load_dwordx4 v37, s[24:27], s29 offen offset:0 lds\n"
      "v_mfma_f32_16x16x32_bf16 v[152:155], a[168:171], a[200:203], v[152:155]\n"
      "v_mfma_f32_16x16x32_bf16 v[184:187], a[172:175], a[200:203], v[184:187]\n"
      "buffer_load_dwordx4 v38, s[24:27], s29 offen offset:1024 lds\n"
      "v_mfma_f32_16x16x32_bf16 v[188:191], a[172:175], a[204:207], v[188:191]\n"
      "v_mfma_f32_16x16x32_bf16 v[156:159], a[168:171], a[204:207], v[156:159]\n"
      "buffer_load_dwordx4 v39, s[24:27], s29 offen offset:2048 lds\n"
      "v_mfma_f32_16x16x32_bf16 v[124:127], a[164:167], a[204:207], v[124:127]\n"
      "v_mfma_f32_16x16x32_bf16 v[92:95], a[160:163], a[204:207], v[92:95]\n"
      "buffer_load_dwordx4 v40, s[24:27], s29 offen offset:3072 lds\n"
      "v_mfma_f32_16x16x32_bf16 v[80:83], a[176:179], a[0:3], v[80:83]\n"
      "s_add_u32 m0, s85, 4096\n"
      "v_mfma_f32_16x16x32_bf16 v[112:115], a[180:183], a[0:3], v[112:115]\n"
      "buffer_load_dwordx4 v41, s[24:27], s29 offen offset:0 lds\n"
      "v_mfma_f32_16x16x32_bf16 v[144:147], a[184:187], a[0:3], v[144:147]\n"
      "v_mfma_f32_16x16x32_bf16 v[176:179], a[188:191], a[0:3], v[176:179]\n"
      "buffer_load_dwordx4 v42, s[24:27], s29 offen offset:1024 lds\n"
      "v_mfma_f32_16x16x32_bf16 v[180:183], a[188:191], a[4:7], v[180:183]\n"
      "v_mfma_f32_16x16x32_bf16 v[148:151], a[184:187], a[4:7], v[148:151]\n"
      "buffer_load_dwordx4 v43, s[24:27], s29 offen offset:2048 lds\n"
      "v_mfma_f32_16x16x32_bf16 v[116:119], a[180:183], a[4:7], v[116:119]\n"
      "v_mfma_f32_16x16x32_bf16 v[84:87], a[176:179], a[4:7], v[84:87]\n"
      "buffer_load_dwordx4 v44, s[24:27], s29 offen offset:3072 lds\n"
      "v_mfma_f32_16x16x32_bf16 v[88:91], a[176:179], a[8:11], v[88:91]\n"
      "v_mfma_f32_16x16x32_bf16 v[120:123], a[180:183], a[8:11], v[120:123]\n"
      "v_mfma_f32_16x16x32_bf16 v[152:155], a[184:187], a[8:11], v[152:155]\n"
      "v_mfma_f32_16x16x32_bf16 v[184:187], a[188:191], a[8:11], v[184:187]\n"
      "v_mfma_f32_16x16x32_bf16 v[188:191], a[188:191], a[12:15], v[188:191]\n"
      "v_cvt_pk_bf16_f32 v208, -v80, -v81\n"
      "v_cvt_pk_bf16_f32 v209, -v82, -v83\n"
      "v_cvt_pk_bf16_f32 v210, -v112, -v113\n"
      "v_mfma_f32_16x16x32_bf16 v[156:159], a[184:187], a[12:15], v[156:159]\n"
      "v_cvt_pk_bf16_f32 v211, -v114, -v115\n"
      "s_nop 0\n"
      "v_permlane16_swap_b32 v208, v210\n"
      "v_mfma_f32_16x16x32_bf16 v[124:127], a[180:183], a[12:15], v[124:127]\n"
      "v_permlane16_swap_b32 v209, v211\n"
      "buffer_store_dwordx4 v[208:211], v58, s[64:67], s74 offen offset:0 sc1\n"
      "v_cvt_pk_bf16_f32 v212, -v144, -v145\n"
      "v_mfma_f32_16x16x32_bf16 v[92:95], a[176:179], a[12:15], v[92:95]\n"
      "v_cvt_pk_bf16_f32 v213, -v146, -v147\n"
      "v_cvt_pk_bf16_f32 v214, -v176, -v177\n"
      "v_cvt_pk_bf16_f32 v215, -v178, -v179\n"
      "s_nop 0\n"
      "v_permlane16_swap_b32 v212, v214\n"
      "v_permlane16_swap_b32 v213, v215\n"
      "buffer_store_dwordx4 v[212:215], v58, s[64:67], s74 offen offset:1024 sc1\n"
      "v_cvt_pk_bf16_f32 v216, -v84, -v85\n"
      "v_cvt_pk_bf16_f32 v217, -v86, -v87\n"
      "v_cvt_pk_bf16_f32 v218, -v116, -v117\n"
      "v_cvt_pk_bf16_f32 v219, -v118, -v119\n"
      "s_nop 0\n"
      "v_permlane16_swap_b32 v216, v218\n"
      "v_permlane16_swap_b32 v217, v219\n"
      "buffer_store_dwordx4 v[216:219], v58, s[64:67], s74 offen offset:2048 sc1\n"
      "v_cvt_pk_bf16_f32 v220, -v148, -v149\n"
      "v_cvt_pk_bf16_f32 v221, -v150, -v151\n"
      "v_cvt_pk_bf16_f32 v222, -v180, -v181\n"
      "v_cvt_pk_bf16_f32 v223, -v182, -v183\n"
      "s_nop 0\n"
      "v_permlane16_swap_b32 v220, v222\n"
      "v_permlane16_swap_b32 v221, v223\n"
      "buffer_store_dwordx4 v[220:223], v58, s[64:67], s74 offen offset:3072 sc1\n"
      "v_cvt_pk_bf16_f32 v224, -v88, -v89\n"
      "v_cvt_pk_bf16_f32 v225, -v90, -v91\n"
      "v_cvt_pk_bf16_f32 v226, -v120, -v121\n"
      "v_cvt_pk_bf16_f32 v227, -v122, -v123\n"
      "s_nop 0\n"
      "v_permlane16_swap_b32 v224, v226\n"
      "v_permlane16_swap_b32 v225, v227\n"
      "buffer_store_dwordx4 v[224:227], v58, s[64:67], s75 offen offset:0 sc1\n"
      "v_cvt_pk_bf16_f32 v228, -v152, -v153\n"
      "v_cvt_pk_bf16_f32 v229, -v154, -v155\n"
      "v_cvt_pk_bf16_f32 v230, -v184, -v185\n"
      "v_cvt_pk_bf16_f32 v231, -v186, -v187\n"
      "s_nop 0\n"
      "v_permlane16_swap_b32 v228, v230\n"
      "v_permlane16_swap_b32 v229, v231\n"
      "buffer_store_dwordx4 v[228:231], v58, s[64:67], s75 offen offset:1024 sc1\n"
      "v_cvt_pk_bf16_f32 v232, -v92, -v93\n"
      "v_cvt_pk_bf16_f32 v233, -v94, -v95\n"
      "v_cvt_pk_bf16_f32 v234, -v124, -v125\n"
      "v_cvt_pk_bf16_f32 v235, -v126, -v127\n"
      "s_nop 0\n"
      "v_permlane16_swap_b32 v232, v234\n"
      "v_permlane16_swap_b32 v233, v235\n"
      "buffer_store_dwordx4 v[232:235], v58, s[64:67], s75 offen offset:2048 sc1\n"
      "v_cvt_pk_bf16_f32 v236, -v156, -v157\n"
      "v_cvt_pk_bf16_f32 v237, -v158, -v159\n"
      "v_cvt_pk_bf16_f32 v238, -v188, -v189\n"
      "v_cvt_pk_bf16_f32 v239, -v190, -v191\n"
      "s_nop 0\n"
      "v_permlane16_swap_b32 v236, v238\n"
      "v_permlane16_swap_b32 v237, v239\n"
      "buffer_store_dwordx4 v[236:239], v58, s[64:67], s75 offen offset:3072 sc1\n"
      "s_nop 1\n"
      "s_lshr_b64 s[10:11], s[14:15], 0\n"
      "s_and_b32 s83, s10, 15\n"
      "s_lshr_b64 s[10:11], s[10:11], 4\n"
      "s_mul_i32 s28, s83, s30\n"
      "s_add_u32 s28, s28, s81\n"
      "s_lshl_b32 s29, s83, 7\n"
      "s_add_u32 s29, s29, s82\n"
      "s_or_b32 s74, s57, s58\n"
      "s_or_b32 s74, s74, s56\n"
      "s_or_b32 s74, s74, s61\n"
      "s_cmp_eq_u32 s74, 0\n"
      "s_cbranch_scc0 Lk_eb_14\n"
      "v_lshlrev_b32 v59, 7, v4\n"
      "s_mov_b32 s84, 0x40000\n"
      "Lk_el_15:\n"
      "s_mov_b32 s74, 131072\n"
      "buffer_load_dwordx2 v[192:193], v59, s[64:67], s74 offen sc1\n"
      "s_mov_b32 s74, 139264\n"
      "buffer_load_dwordx2 v[194:195], v59, s[64:67], s74 offen sc1\n"
      "s_mov_b32 s74, 147456\n"
      "buffer_load_dwordx2 v[196:197], v59, s[64:67], s74 offen sc1\n"
      "s_mov_b32 s74, 155648\n"
      "buffer_load_dwordx2 v[198:199], v59, s[64:67], s74 offen sc1\n"
      "s_waitcnt vmcnt(0)\n"
      "s_mov_b64 s[72:73], 0\n"
      "v_cmp_ne_u64 vcc, s[54:55], v[192:193]\n"
      "s_or_b64 s[72:73], s[72:73], vcc\n"
      "v_cmp_ne_u64 vcc, s[54:55], v[194:195]\n"
      "s_or_b64 s[72:73], s[72:73], vcc\n"
      "v_cmp_ne_u64 vcc, s[54:55], v[196:197]\n"
      "s_or_b64 s[72:73], s[72:73], vcc\n"
      "v_cmp_ne_u64 vcc, s[54:55], v[198:199]\n"
      "s_or_b64 s[72:73], s[72:73], vcc\n"
      "s_cbranch_scc0 Lk_eo_16\n"
      "s_sleep 1\n"
      "s_sub_u32 s84, s84, 1\n"
      "s_cmp_lg_u32 s84, 0\n"
      "s_cbranch_scc1 Lk_el_15\n"
      "Lk_eo_16:\n"
      "s_mov_b32 s74, 0\n"
      "v_mov_b32 v6, s54\n"
      "v_mov_b32 v7, s55\n"
      "v_min_u32 v5, 1, v4\n"
      "v_lshlrev_b32 v5, 30, v5\n"
      "buffer_store_dwordx2 v[6:7], v5, s[64:67], s74 offen sc1\n"
      "Lk_eb_14:\n"
      "s_mov_b32 s84, 0x40000\n"
      "Lk_pl_18:\n"
      "buffer_load_dwordx2 v[56:57], v55, s[64:67], s80 offen sc1\n"
      "s_waitcnt vmcnt(0)\n"
      "v_cmp_ne_u64 vcc, s[54:55], v[56:57]\n"
      "s_cbranch_vccz Lk_pd_17\n"
      "s_sleep 1\n"
      "s_sub_u32 s84, s84, 1\n"
      "s_cmp_lg_u32 s84, 0\n"
      "s_cbranch_scc1 Lk_pl_18\n"
      "Lk_pd_17:\n"
      "s_add_u32 m0, s92, 81920\n"
      "s_nop 0\n"
      "buffer_load_dwordx4 v33, s[64:67], s28 offen offset:0 sc1 lds\n"
      "buffer_load_dwordx4 v34, s[64:67], s28 offen offset:1024 sc1 lds\n"
      "buffer_load_dwordx4 v35, s[64:67], s28 offen offset:2048 sc1 lds\n"
      "buffer_load_dwordx4 v36, s[64:67], s28 offen offset:3072 sc1 lds\n"
      "s_and_b32 s83, s10, 15\n"
      "s_lshr_b64 s[10:11], s[10:11], 4\n"
      "s_mul_i32 s28, s83, s30\n"
      "s_add_u32 s28, s28, s81\n"
      "s_lshl_b32 s29, s83, 7\n"
      "s_add_u32 s29, s29, s82\n"
      "s_add_u32 m0, s92, 131072\n"
      "s_nop 0\n"
      "buffer_load_dwordx4 v33, s[64:67], s28 offen offset:0 sc1 lds\n"
      "buffer_load_dwordx4 v34, s[64:67], s28 offen offset:1024 sc1 lds\n"
      "buffer_load_dwordx4 v35, s[64:67], s28 offen offset:2048 sc1 lds\n"
      "buffer_load_dwordx4 v36, s[64:67], s28 offen offset:3072 sc1 lds\n"
      "s_and_b32 s83, s10, 15\n"
      "s_lshr_b64 s[10:11], s[10:11], 4\n"
      "s_mul_i32 s28, s83, s30\n"
      "s_add_u32 s28, s28, s81\n"
      "s_lshl_b32 s29, s83, 7\n"
      "s_add_u32 s29, s29, s82\n"
      "s_add_u32 m0, s92, 32768\n"
      "s_nop 0\n"
      "buffer_load_dwordx4 v33, s[64:67], s28 offen offset:0 sc1 lds\n"
      "buffer_load_dwordx4 v34, s[64:67], s28 offen offset:1024 sc1 lds\n"
      "buffer_load_dwordx4 v35, s[64:67], s28 offen offset:2048 sc1 lds\n"
      "buffer_load_dwordx4 v36, s[64:67], s28 offen offset:3072 sc1 lds\n"
      "s_and_b32 s83, s10, 15\n"
      "s_lshr_b64 s[10:11], s[10:11], 4\n"
      "s_mul_i32 s28, s83, s30\n"
      "s_add_u32 s28, s28, s81\n"
      "s_lshl_b32 s29, s83, 7\n"
      "s_add_u32 s29, s29, s82\n"
      "s_waitcnt vmcnt(8)\n"
      "s_barrier\n"
      "ds_read_b128 a[0:3], v45 offset:32768\n"
      "ds_read_b128 a[16:19], v47 offset:0\n"
      "ds_read_b128 a[20:23], v47 offset:2048\n"
      "ds_read_b128 a[24:27], v47 offset:4096\n"
      "ds_read_b128 a[28:31], v47 offset:6144\n"
      "ds_read_b128 a[32:35], v47 offset:8192\n"
      "ds_read_b128 a[36:39], v47 offset:10240\n"
      "ds_read_b128 a[40:43], v47 offset:12288\n"
      "ds_read_b128 a[44:47], v47 offset:14336\n"
      "ds_read_b128 a[4:7], v45 offset:34816\n"
      "ds_read_b128 a[8:11], v45 offset:36864\n"
      "ds_read_b128 a[12:15], v45 offset:38912\n"
      "s_waitcnt lgkmcnt(0)\n"
      "v_mfma_f32_16x16x32_bf16 v[64:67], a[0:3], a[16:19], 0\n"
      "ds_read_b128 a[48:51], v46 offset:32768\n"
      "v_mfma_f32_16x16x32_bf16 v[68:71], a[0:3], a[20:23], 0\n"
      "ds_read_b128 a[64:67], v48 offset:0\n"
      "v_mfma_f32_16x16x32_bf16 v[72:75], a[0:3], a[24:27], 0\n"
      "v_mfma_f32_16x16x32_bf16 v[76:79], a[0:3], a[28:31], 0\n"
      "ds_read_b128 a[68:71], v48 offset:2048\n"
      "v_mfma_f32_16x16x32_bf16 v[80:83], a[0:3], a[32:35], 0\n"
      "v_mfma_f32_16x16x32_bf16 v[84:87], a[0:3], a[36:39], 0\n"
      "ds_read_b128 a[72:75], v48 offset:4096\n"
      "v_mfma_f32_16x16x32_bf16 v[88:91], a[0:3], a[40:43], 0\n"
      "ds_read_b128 a[76:79], v48 offset:6144\n"
      "v_mfma_f32_16x16x32_bf16 v[92:95], a[0:3], a[44:47], 0\n"
      "v_mfma_f32_16x16x32_bf16 v[124:127], a[4:7], a[44:47], 0\n"
      "ds_read_b128 a[80:83], v48 offset:8192\n"
      "v_mfma_f32_16x16x32_bf16 v[120:123], a[4:7], a[40:43], 0\n"
      "v_mfma_f32_16x16x32_bf16 v[116:119], a[4:7], a[36:39], 0\n"
      "ds_read_b128 a[84:87], v48 offset:10240\n"
      "v_mfma_f32_16x16x32_bf16 v[112:115], a[4:7], a[32:35], 0\n"
      "ds_read_b128 a[88:91], v48 offset:12288\n"
      "v_mfma_f32_16x16x32_bf16 v[108:111], a[4:7], a[28:31], 0\n"
      "v_mfma_f32_16x16x32_bf16 v[104:107], a[4:7], a[24:27], 0\n"
      "ds_read_b128 a[92:95], v48 offset:14336\n"
      "v_mfma_f32_16x16x32_bf16 v[100:103], a[4:7], a[20:23], 0\n"
      "v_mfma_f32_16x16x32_bf16 v[96:99], a[4:7], a[16:19], 0\n"
      "ds_read_b128 a[52:55], v46 offset:34816\n"
      "v_mfma_f32_16x16x32_bf16 v[128:131], a[8:11], a[16:19], 0\n"
      "ds_read_b128 a[56:59], v46 offset:36864\n"
      "v_mfma_f32_16x16x32_bf16 v[132:135], a[8:11], a[20:23], 0\n"
      "v_mfma_f32_16x16x32_bf16 v[136:139], a[8:11], a[24:27], 0\n"
      "ds_read_b128 a[60:63], v46 offset:38912\n"
      "v_mfma_f32_16x16x32_bf16 v[140:143], a[8:11], a[28:31], 0\n"
      "v_mfma_f32_16x16x32_bf16 v[144:147], a[8:11], a[32:35], 0\n"
      "v_mfma_f32_16x16x32_bf16 v[148:151], a[8:11], a[36:39], 0\n"
      "v_mfma_f32_16x16x32_bf16 v[152:155], a[8:11], a[40:43], 0\n"
      "v_mfma_f32_16x16x32_bf16 v[156:159], a[8:11], a[44:47], 0\n"
      "v_mfma_f32_16x16x32_bf16 v[188:191], a[12:15], a[44:47], 0\n"
      "v_mfma_f32_16x16x32_bf16 v[184:187], a[12:15], a[40:43], 0\n"
      "s_add_u32 s96, s94, s85\n"
      "s_add_u32 s95, s94, s92\n"
      "v_mfma_f32_16x16x32_bf16 v[180:183], a[12:15], a[36:39], 0\n"
      "s_add_u32 s95, s95, 32768\n"
      "s_cmp_eq_u32 s94, 98304\n"
      "v_mfma_f32_16x16x32_bf16 v[176:179], a[12:15], a[32:35], 0\n"
      "s_cselect_b32 s97, 0xfffe8000, s99\n"
      "s_add_u32 s94, s94, s97\n"
      "v_mfma_f32_16x16x32_bf16 v[172:175], a[12:15], a[28:31], 0\n"
      "v_add_u32 v45, s97, v45\n"
      "v_add_u32 v46, s97, v46\n"
      "v_mfma_f32_16x16x32_bf16 v[168:171], a[12:15], a[24:27], 0\n"
      "v_add_u32 v47, s97, v47\n"
      "v_add_u32 v48, s97, v48\n"
      "v_mfma_f32_16x16x32_bf16 v[164:167], a[12:15], a[20:23], 0\n"
      "v_mfma_f32_16x16x32_bf16 v[160:163], a[12:15], a[16:19], 0\n"
      "s_waitcnt vmcnt(4)\n"
      "s_add_u32 s76, s76, 32768\n"
      "s_add_u32 s80, s80, 32768\n"
      "s_cmp_eq_u32 s61, 0\n"
      "s_cbranch_scc0 Lk_pub_19\n"
      "v_mov_b32 v6, s54\n"
      "v_mov_b32 v7, s55\n"
      "v_min_u32 v5, 1, v4\n"
      "v_lshlrev_b32 v5, 30, v5\n"
      "buffer_store_dwordx2 v[6:7], v5, s[64:67], s76 offen sc1\n"
      "Lk_pub_19:\n"
      "s_mov_b32 s93, 11\n"
      "s_call_b64 s[34:35], Lk_nt\n"
      "buffer_load_dwordx2 v[56:57], v55, s[64:67], s80 offen sc1\n"
      "s_mov_b32 s93, 2\n"
      "s_call_b64 s[34:35], Lk_nt\n"
      "v_cmp_ne_u64 vcc, s[54:55], v[56:57]\n"
      "s_cbranch_vccz Lk_pd_20\n"
      "s_mov_b32 s84, 0x40000\n"
      "Lk_pl_21:\n"
      "buffer_load_dwordx2 v[56:57], v55, s[64:67], s80 offen sc1\n"
      "s_waitcnt vmcnt(0)\n"
      "v_cmp_ne_u64 vcc, s[54:55], v[56:57]\n"
      "s_cbranch_vccz Lk_pd_20\n"
      "s_sleep 1\n"
      "s_sub_u32 s84, s84, 1\n"
      "s_cmp_lg_u32 s84, 0\n"
      "s_cbranch_scc1 Lk_pl_21\n"
      "Lk_pd_20:\n"
      "s_mov_b32 s24, s40\n"
      "s_and_b32 s25, s41, 0xffff\n"
      "s_mov_b32 s81, s78\n"
      "s_mov_b32 s82, s79\n"
      "s_lshr_b64 s[10:11], s[16:17], 0\n"
      "s_and_b32 s83, s10, 15\n"
      "s_lshr_b64 s[10:11], s[10:11], 4\n"
      "s_mul_i32 s28, s83, s30\n"
      "s_add_u32 s28, s28, s81\n"
      "s_lshl_b32 s29, s83, 7\n"
      "s_add_u32 s29, s29, s82\n"
      "s_mov_b32 s93, 5\n"
      "s_call_b64 s[34:35], Lk_nt\n"
      "s_lshl_b32 s80, s57, 11\n"
      "s_add_u32 s80, s80, 180224\n"
      "buffer_load_dwordx2 v[56:57], v55, s[64:67], s80 offen sc1\n"
      "s_mov_b32 s93, 2\n"
      "s_call_b64 s[34:35], Lk_nt\n"
      "v_cmp_ne_u64 vcc, s[54:55], v[56:57]\n"
      "s_cbranch_vccz Lk_pd_22\n"
      "s_mov_b32 s84, 0x40000\n"
      "Lk_pl_23:\n"
      "buffer_load_dwordx2 v[56:57], v55, s[64:67], s80 offen sc1\n"
      "s_waitcnt vmcnt(0)\n"
      "v_cmp_ne_u64 vcc, s[54:55], v[56:57]\n"
      "s_cbranch_vccz Lk_pd_22\n"
      "s_sleep 1\n"
      "s_sub_u32 s84, s84, 1\n"
      "s_cmp_lg_u32 s84, 0\n"
      "s_cbranch_scc1 Lk_pl_23\n"
      "Lk_pd_22:\n"
      "s_mov_b32 s93, 10\n"
      "s_branch Lk_ffn\n"
      "Lk_nt:\n"
      "s_waitcnt lgkmcnt(0)\n"
      "v_mfma_f32_16x16x32_bf16 v[64:67], a[48:51], a[64:67], v[64:67]\n"
      "s_waitcnt vmcnt(12)\n"
      "s_barrier\n"
      "v_mfma_f32_16x16x32_bf16 v[68:71], a[48:51], a[68:71], v[68:71]\n"
      "ds_read_b128 a[0:3], v45 offset:32768\n"
      "v_mfma_f32_16x16x32_bf16 v[72:75], a[48:51], a[72:75], v[72:75]\n"
      "ds_read_b128 a[16:19], v47 offset:0\n"
      "s_mov_b32 m0, s95\n"
      "v_mfma_f32_16x16x32_bf16 v[76:79], a[48:51], a[76:79], v[76:79]\n"
      "buffer_load_dwordx4 v33, s[64:67], s28 offen offset:0 sc1 lds\n"
      "v_mfma_f32_16x16x32_bf16 v[80:83], a[48:51], a[80:83], v[80:83]\n"
      "ds_read_b128 a[20:23], v47 offset:2048\n"
      "v_mfma_f32_16x16x32_bf16 v[84:87], a[48:51], a[84:87], v[84:87]\n"
      "ds_read_b128 a[24:27], v47 offset:4096\n"
      "v_mfma_f32_16x16x32_bf16 v[88:91], a[48:51], a[88:91], v[88:91]\n"
      "ds_read_b128 a[28:31], v47 offset:6144\n"
      "v_mfma_f32_16x16x32_bf16 v[92:95], a[48:51], a[92:95], v[92:95]\n"
      "ds_read_b128 a[32:35], v47 offset:8192\n"
      "v_mfma_f32_16x16x32_bf16 v[124:127], a[52:55], a[92:95], v[124:127]\n"
      "buffer_load_dwordx4 v34, s[64:67], s28 offen offset:1024 sc1 lds\n"
      "v_mfma_f32_16x16x32_bf16 v[120:123], a[52:55], a[88:91], v[120:123]\n"
      "ds_read_b128 a[36:39], v47 offset:10240\n"
      "v_mfma_f32_16x16x32_bf16 v[116:119], a[52:55], a[84:87], v[116:119]\n"
      "ds_read_b128 a[40:43], v47 offset:12288\n"
      "v_mfma_f32_16x16x32_bf16 v[112:115], a[52:55], a[80:83], v[112:115]\n"
      "ds_read_b128 a[44:47], v47 offset:14336\n"
      "v_mfma_f32_16x16x32_bf16 v[108:111], a[52:55], a[76:79], v[108:111]\n"
      "ds_read_b128 a[4:7], v45 offset:34816\n"
      "v_mfma_f32_16x16x32_bf16 v[104:107], a[52:55], a[72:75], v[104:107]\n"
      "buffer_load_dwordx4 v35, s[64:67], s28 offen offset:2048 sc1 lds\n"
      "v_mfma_f32_16x16x32_bf16 v[100:103], a[52:55], a[68:71], v[100:103]\n"
      "ds_read_b128 a[8:11], v45 offset:36864\n"
      "v_mfma_f32_16x16x32_bf16 v[96:99], a[52:55], a[64:67], v[96:99]\n"
      "ds_read_b128 a[12:15], v45 offset:38912\n"
      "v_mfma_f32_16x16x32_bf16 v[128:131], a[56:59], a[64:67], v[128:131]\n"
      "v_mfma_f32_16x16x32_bf16 v[132:135], a[56:59], a[68:71], v[132:135]\n"
      "v_mfma_f32_16x16x32_bf16 v[136:139], a[56:59], a[72:75], v[136:139]\n"
      "buffer_load_dwordx4 v36, s[64:67], s28 offen offset:3072 sc1 lds\n"
      "v_mfma_f32_16x16x32_bf16 v[140:143], a[56:59], a[76:79], v[140:143]\n"
      "s_and_b32 s83, s10, 15\n"
      "v_mfma_f32_16x16x32_bf16 v[144:147], a[56:59], a[80:83], v[144:147]\n"
      "s_lshr_b64 s[10:11], s[10:11], 4\n"
      "v_mfma_f32_16x16x32_bf16 v[148:151], a[56:59], a[84:87], v[148:151]\n"
      "s_mul_i32 s28, s83, s30\n"
      "v_mfma_f32_16x16x32_bf16 v[152:155], a[56:59], a[88:91], v[152:155]\n"
      "s_add_u32 s28, s28, s81\n"
      "s_add_u32 m0, s96, 0\n"
      "v_mfma_f32_16x16x32_bf16 v[156:159], a[56:59], a[92:95], v[156:159]\n"
      "buffer_load_dwordx4 v37, s[24:27], s29 offen offset:0 lds\n"
      "v_mfma_f32_16x16x32_bf16 v[188:191], a[60:63], a[92:95], v[188:191]\n"
      "v_mfma_f32_16x16x32_bf16 v[184:187], a[60:63], a[88:91], v[184:187]\n"
      "v_mfma_f32_16x16x32_bf16 v[180:183], a[60:63], a[84:87], v[180:183]\n"
      "v_mfma_f32_16x16x32_bf16 v[176:179], a[60:63], a[80:83], v[176:179]\n"
      "v_mfma_f32_16x16x32_bf16 v[172:175], a[60:63], a[76:79], v[172:175]\n"
      "buffer_load_dwordx4 v38, s[24:27], s29 offen offset:1024 lds\n"
      "v_mfma_f32_16x16x32_bf16 v[168:171], a[60:63], a[72:75], v[168:171]\n"
      "v_mfma_f32_16x16x32_bf16 v[164:167], a[60:63], a[68:71], v[164:167]\n"
      "v_mfma_f32_16x16x32_bf16 v[160:163], a[60:63], a[64:67], v[160:163]\n"
      "s_waitcnt lgkmcnt(0)\n"
      "v_mfma_f32_16x16x32_bf16 v[64:67], a[0:3], a[16:19], v[64:67]\n"
      "ds_read_b128 a[48:51], v46 offset:32768\n"
      "v_mfma_f32_16x16x32_bf16 v[68:71], a[0:3], a[20:23], v[68:71]\n"
      "ds_read_b128 a[64:67], v48 offset:0\n"
      "v_mfma_f32_16x16x32_bf16 v[72:75], a[0:3], a[24:27], v[72:75]\n"
      "ds_read_b128 a[68:71], v48 offset:2048\n"
      "v_mfma_f32_16x16x32_bf16 v[76:79], a[0:3], a[28:31], v[76:79]\n"
      "buffer_load_dwordx4 v39, s[24:27], s29 offen offset:2048 lds\n"
      "v_mfma_f32_16x16x32_bf16 v[80:83], a[0:3], a[32:35], v[80:83]\n"
      "ds_read_b128 a[72:75], v48 offset:4096\n"
      "v_mfma_f32_16x16x32_bf16 v[84:87], a[0:3], a[36:39], v[84:87]\n"
      "ds_read_b128 a[76:79], v48 offset:6144\n"
      "v_mfma_f32_16x16x32_bf16 v[88:91], a[0:3], a[40:43], v[88:91]\n"
      "ds_read_b128 a[80:83], v48 offset:8192\n"
      "v_mfma_f32_16x16x32_bf16 v[92:95], a[0:3], a[44:47], v[92:95]\n"
      "ds_read_b128 a[84:87], v48 offset:10240\n"
      "v_mfma_f32_16x16x32_bf16 v[124:127], a[4:7], a[44:47], v[124:127]\n"
      "buffer_load_dwordx4 v40, s[24:27], s29 offen offset:3072 lds\n"
      "v_mfma_f32_16x16x32_bf16 v[120:123], a[4:7], a[40:43], v[120:123]\n"
      "ds_read_b128 a[88:91], v48 offset:12288\n"
      "v_mfma_f32_16x16x32_bf16 v[116:119], a[4:7], a[36:39], v[116:119]\n"
      "ds_read_b128 a[92:95], v48 offset:14336\n"
      "v_mfma_f32_16x16x32_bf16 v[112:115], a[4:7], a[32:35], v[112:115]\n"
      "ds_read_b128 a[52:55], v46 offset:34816\n"
      "v_mfma_f32_16x16x32_bf16 v[108:111], a[4:7], a[28:31], v[108:111]\n"
      "ds_read_b128 a[56:59], v46 offset:36864\n"
      "s_add_u32 m0, s96, 4096\n"
      "v_mfma_f32_16x16x32_bf16 v[104:107], a[4:7], a[24:27], v[104:107]\n"
      "buffer_load_dwordx4 v41, s[24:27], s29 offen offset:0 lds\n"
      "v_mfma_f32_16x16x32_bf16 v[100:103], a[4:7], a[20:23], v[100:103]\n"
      "ds_read_b128 a[60:63], v46 offset:38912\n"
      "v_mfma_f32_16x16x32_bf16 v[96:99], a[4:7], a[16:19], v[96:99]\n"
      "s_add_u32 s96, s94, s85\n"
      "v_mfma_f32_16x16x32_bf16 v[128:131], a[8:11], a[16:19], v[128:131]\n"
      "s_add_u32 s95, s94, s92\n"
      "s_add_u32 s95, s95, 32768\n"
      "v_mfma_f32_16x16x32_bf16 v[132:135], a[8:11], a[20:23], v[132:135]\n"
      "s_cmp_eq_u32 s94, 98304\n"
      "s_cselect_b32 s97, 0xfffe8000, s99\n"
      "s_add_u32 s94, s94, s97\n"
      "v_mfma_f32_16x16x32_bf16 v[136:139], a[8:11], a[24:27], v[136:139]\n"
      "buffer_load_dwordx4 v42, s[24:27], s29 offen offset:1024 lds\n"
      "v_mfma_f32_16x16x32_bf16 v[140:143], a[8:11], a[28:31], v[140:143]\n"
      "v_add_u32 v45, s97, v45\n"
      "v_add_u32 v46, s97, v46\n"
      "v_mfma_f32_16x16x32_bf16 v[144:147], a[8:11], a[32:35], v[144:147]\n"
      "v_add_u32 v47, s97, v47\n"
      "v_add_u32 v48, s97, v48\n"
      "v_mfma_f32_16x16x32_bf16 v[148:151], a[8:11], a[36:39], v[148:151]\n"
      "v_mfma_f32_16x16x32_bf16 v[152:155], a[8:11], a[40:43], v[152:155]\n"
      "v_mfma_f32_16x16x32_bf16 v[156:159], a[8:11], a[44:47], v[156:159]\n"
      "buffer_load_dwordx4 v43, s[24:27], s29 offen offset:2048 lds\n"
      "v_mfma_f32_16x16x32_bf16 v[188:191], a[12:15], a[44:47], v[188:191]\n"
      "v_mfma_f32_16x16x32_bf16 v[184:187], a[12:15], a[40:43], v[184:187]\n"
      "v_mfma_f32_16x16x32_bf16 v[180:183], a[12:15], a[36:39], v[180:183]\n"
      "v_mfma_f32_16x16x32_bf16 v[176:179], a[12:15], a[32:35], v[176:179]\n"
      "v_mfma_f32_16x16x32_bf16 v[172:175], a[12:15], a[28:31], v[172:175]\n"
      "buffer_load_dwordx4 v44, s[24:27], s29 offen offset:3072 lds\n"
      "v_mfma_f32_16x16x32_bf16 v[168:171], a[12:15], a[24:27], v[168:171]\n"
      "s_lshl_b32 s29, s83, 7\n"
      "s_add_u32 s29, s29, s82\n"
      "v_mfma_f32_16x16x32_bf16 v[164:167], a[12:15], a[20:23], v[164:167]\n"
      "v_mfma_f32_16x16x32_bf16 v[160:163], a[12:15], a[16:19], v[160:163]\n"
      "s_sub_u32 s93, s93, 1\n"
      "s_cmp_lg_u32 s93, 0\n"
      "s_cbranch_scc1 Lk_nt\n"
      "Lk_nt_x:\n"
      "s_setpc_b64 s[34:35]\n"
      "Lk_ffn:\n"
      "s_lshr_b64 s[10:11], s[18:19], 0\n"
      "s_and_b32 s83, s10, 15\n"
      "s_lshr_b64 s[10:11], s[10:11], 4\n"
      "s_lshl_b32 s28, s83, 17\n"
      "s_add_u32 s28, s28, s91\n"
      "s_lshl_b32 s29, s83, 15\n"
      "s_add_u32 s29, s29, s98\n"
      "s_waitcnt lgkmcnt(0)\n"
      "v_mfma_f32_16x16x32_bf16 v[64:67], a[48:51], a[64:67], v[64:67]\n"
      "s_waitcnt vmcnt(12)\n"
      "s_barrier\n"
      "v_mfma_f32_16x16x32_bf16 v[68:71], a[48:51], a[68:71], v[68:71]\n"
      "ds_read_b128 a[0:3], v45 offset:32768\n"
      "v_mfma_f32_16x16x32_bf16 v[72:75], a[48:51], a[72:75], v[72:75]\n"
      "ds_read_b128 a[16:19], v47 offset:0\n"
      "s_mov_b32 m0, s95\n"
      "v_mfma_f32_16x16x32_bf16 v[76:79], a[48:51], a[76:79], v[76:79]\n"
      "buffer_load_dwordx4 v1, s[20:23], s28 offen offset:0 lds\n"
      "v_mfma_f32_16x16x32_bf16 v[80:83], a[48:51], a[80:83], v[80:83]\n"
      "ds_read_b128 a[20:23], v47 offset:2048\n"
      "v_mfma_f32_16x16x32_bf16 v[84:87], a[48:51], a[84:87], v[84:87]\n"
      "ds_read_b128 a[24:27], v47 offset:4096\n"
      "v_mfma_f32_16x16x32_bf16 v[88:91], a[48:51], a[88:91], v[88:91]\n"
      "ds_read_b128 a[28:31], v47 offset:6144\n"
      "v_mfma_f32_16x16x32_bf16 v[92:95], a[48:51], a[92:95], v[92:95]\n"
      "ds_read_b128 a[32:35], v47 offset:8192\n"
      "v_mfma_f32_16x16x32_bf16 v[124:127], a[52:55], a[92:95], v[124:127]\n"
      "buffer_load_dwordx4 v2, s[20:23], s28 offen offset:1024 lds\n"
      "v_mfma_f32_16x16x32_bf16 v[120:123], a[52:55], a[88:91], v[120:123]\n"
      "ds_read_b128 a[36:39], v47 offset:10240\n"
      "v_mfma_f32_16x16x32_bf16 v[116:119], a[52:55], a[84:87], v[116:119]\n"
      "ds_read_b128 a[40:43], v47 offset:12288\n"
      "v_mfma_f32_16x16x32_bf16 v[112:115], a[52:55], a[80:83], v[112:115]\n"
      "ds_read_b128 a[44:47], v47 offset:14336\n"
      "v_mfma_f32_16x16x32_bf16 v[108:111], a[52:55], a[76:79], v[108:111]\n"
      "ds_read_b128 a[4:7], v45 offset:34816\n"
      "v_mfma_f32_16x16x32_bf16 v[104:107], a[52:55], a[72:75], v[104:107]\n"
      "buffer_load_dwordx4 v3, s[20:23], s28 offen offset:2048 lds\n"
      "v_mfma_f32_16x16x32_bf16 v[100:103], a[52:55], a[68:71], v[100:103]\n"
      "ds_read_b128 a[8:11], v45 offset:36864\n"
      "v_mfma_f32_16x16x32_bf16 v[96:99], a[52:55], a[64:67], v[96:99]\n"
      "ds_read_b128 a[12:15], v45 offset:38912\n"
      "v_mfma_f32_16x16x32_bf16 v[128:131], a[56:59], a[64:67], v[128:131]\n"
      "v_mfma_f32_16x16x32_bf16 v[132:135], a[56:59], a[68:71], v[132:135]\n"
      "v_mfma_f32_16x16x32_bf16 v[136:139], a[56:59], a[72:75], v[136:139]\n"
      "buffer_load_dwordx4 v63, s[20:23], s28 offen offset:3072 lds\n"
      "v_mfma_f32_16x16x32_bf16 v[140:143], a[56:59], a[76:79], v[140:143]\n"
      "s_and_b32 s83, s10, 15\n"
      "v_mfma_f32_16x16x32_bf16 v[144:147], a[56:59], a[80:83], v[144:147]\n"
      "s_lshr_b64 s[10:11], s[10:11], 4\n"
      "v_mfma_f32_16x16x32_bf16 v[148:151], a[56:59], a[84:87], v[148:151]\n"
      "s_lshl_b32 s28, s83, 17\n"
      "v_mfma_f32_16x16x32_bf16 v[152:155], a[56:59], a[88:91], v[152:155]\n"
      "s_add_u32 s28, s28, s91\n"
      "s_add_u32 m0, s96, 0\n"
      "v_mfma_f32_16x16x32_bf16 v[156:159], a[56:59], a[92:95], v[156:159]\n"
      "buffer_load_dwordx4 v23, s[64:67], s29 offen offset:0 sc1 lds\n"
      "v_mfma_f32_16x16x32_bf16 v[188:191], a[60:63], a[92:95], v[188:191]\n"
      "v_mfma_f32_16x16x32_bf16 v[184:187], a[60:63], a[88:91], v[184:187]\n"
      "v_mfma_f32_16x16x32_bf16 v[180:183], a[60:63], a[84:87], v[180:183]\n"
      "v_mfma_f32_16x16x32_bf16 v[176:179], a[60:63], a[80:83], v[176:179]\n"
      "v_mfma_f32_16x16x32_bf16 v[172:175], a[60:63], a[76:79], v[172:175]\n"
      "buffer_load_dwordx4 v23, s[64:67], s29 offen offset:1024 sc1 lds\n"
      "v_mfma_f32_16x16x32_bf16 v[168:171], a[60:63], a[72:75], v[168:171]\n"
      "v_mfma_f32_16x16x32_bf16 v[164:167], a[60:63], a[68:71], v[164:167]\n"
      "v_mfma_f32_16x16x32_bf16 v[160:163], a[60:63], a[64:67], v[160:163]\n"
      "s_waitcnt lgkmcnt(0)\n"
      "v_mfma_f32_16x16x32_bf16 v[64:67], a[0:3], a[16:19], v[64:67]\n"
      "ds_read_b128 a[48:51], v46 offset:32768\n"
      "v_mfma_f32_16x16x32_bf16 v[68:71], a[0:3], a[20:23], v[68:71]\n"
      "ds_read_b128 a[64:67], v48 offset:0\n"
      "v_mfma_f32_16x16x32_bf16 v[72:75], a[0:3], a[24:27], v[72:75]\n"
      "ds_read_b128 a[68:71], v48 offset:2048\n"
      "v_mfma_f32_16x16x32_bf16 v[76:79], a[0:3], a[28:31], v[76:79]\n"
      "buffer_load_dwordx4 v23, s[64:67], s29 offen offset:2048 sc1 lds\n"
      "v_mfma_f32_16x16x32_bf16 v[80:83], a[0:3], a[32:35], v[80:83]\n"
      "ds_read_b128 a[72:75], v48 offset:4096\n"
      "v_mfma_f32_16x16x32_bf16 v[84:87], a[0:3], a[36:39], v[84:87]\n"
      "ds_read_b128 a[76:79], v48 offset:6144\n"
      "v_mfma_f32_16x16x32_bf16 v[88:91], a[0:3], a[40:43], v[88:91]\n"
      "ds_read_b128 a[80:83], v48 offset:8192\n"
      "v_mfma_f32_16x16x32_bf16 v[92:95], a[0:3], a[44:47], v[92:95]\n"
      "ds_read_b128 a[84:87], v48 offset:10240\n"
      "v_mfma_f32_16x16x32_bf16 v[124:127], a[4:7], a[44:47], v[124:127]\n"
      "buffer_load_dwordx4 v23, s[64:67], s29 offen offset:3072 sc1 lds\n"
      "v_mfma_f32_16x16x32_bf16 v[120:123], a[4:7], a[40:43], v[120:123]\n"
      "ds_read_b128 a[88:91], v48 offset:12288\n"
      "v_mfma_f32_16x16x32_bf16 v[116:119], a[4:7], a[36:39], v[116:119]\n"
      "ds_read_b128 a[92:95], v48 offset:14336\n"
      "v_mfma_f32_16x16x32_bf16 v[112:115], a[4:7], a[32:35], v[112:115]\n"
      "ds_read_b128 a[52:55], v46 offset:34816\n"
      "v_mfma_f32_16x16x32_bf16 v[108:111], a[4:7], a[28:31], v[108:111]\n"
      "ds_read_b128 a[56:59], v46 offset:36864\n"
      "s_add_u32 m0, s96, 4096\n"
      "v_mfma_f32_16x16x32_bf16 v[104:107], a[4:7], a[24:27], v[104:107]\n"
      "buffer_load_dwordx4 v24, s[64:67], s29 offen offset:0 sc1 lds\n"
      "v_mfma_f32_16x16x32_bf16 v[100:103], a[4:7], a[20:23], v[100:103]\n"
      "ds_read_b128 a[60:63], v46 offset:38912\n"
      "v_mfma_f32_16x16x32_bf16 v[96:99], a[4:7], a[16:19], v[96:99]\n"
      "s_add_u32 s96, s94, s85\n"
      "v_mfma_f32_16x16x32_bf16 v[128:131], a[8:11], a[16:19], v[128:131]\n"
      "s_add_u32 s95, s94, s92\n"
      "s_add_u32 s95, s95, 32768\n"
      "v_mfma_f32_16x16x32_bf16 v[132:135], a[8:11], a[20:23], v[132:135]\n"
      "s_cmp_eq_u32 s94, 98304\n"
      "s_cselect_b32 s97, 0xfffe8000, s99\n"
      "s_add_u32 s94, s94, s97\n"
      "v_mfma_f32_16x16x32_bf16 v[136:139], a[8:11], a[24:27], v[136:139]\n"
      "buffer_load_dwordx4 v24, s[64:67], s29 offen offset:1024 sc1 lds\n"
      "v_mfma_f32_16x16x32_bf16 v[140:143], a[8:11], a[28:31], v[140:143]\n"
      "v_add_u32 v45, s97, v45\n"
      "v_add_u32 v46, s97, v46\n"
      "v_mfma_f32_16x16x32_bf16 v[144:147], a[8:11], a[32:35], v[144:147]\n"
      "v_add_u32 v47, s97, v47\n"
      "v_add_u32 v48, s97, v48\n"
      "v_mfma_f32_16x16x32_bf16 v[148:151], a[8:11], a[36:39], v[148:151]\n"
      "v_mfma_f32_16x16x32_bf16 v[152:155], a[8:11], a[40:43], v[152:155]\n"
      "v_mfma_f32_16x16x32_bf16 v[156:159], a[8:11], a[44:47], v[156:159]\n"
      "buffer_load_dwordx4 v24, s[64:67], s29 offen offset:2048 sc1 lds\n"
      "v_mfma_f32_16x16x32_bf16 v[188:191], a[12:15], a[44:47], v[188:191]\n"
      "v_mfma_f32_16x16x32_bf16 v[184:187], a[12:15], a[40:43], v[184:187]\n"
      "v_mfma_f32_16x16x32_bf16 v[180:183], a[12:15], a[36:39], v[180:183]\n"
      "v_mfma_f32_16x16x32_bf16 v[176:179], a[12:15], a[32:35], v[176:179]\n"
      "v_mfma_f32_16x16x32_bf16 v[172:175], a[12:15], a[28:31], v[172:175]\n"
      "buffer_load_dwordx4 v24, s[64:67], s29 offen offset:3072 sc1 lds\n"
      "v_mfma_f32_16x16x32_bf16 v[168:171], a[12:15], a[24:27], v[168:171]\n"
      "s_lshl_b32 s29, s83, 15\n"
      "s_add_u32 s29, s29, s98\n"
      "v_mfma_f32_16x16x32_bf16 v[164:167], a[12:15], a[20:23], v[164:167]\n"
      "v_mfma_f32_16x16x32_bf16 v[160:163], a[12:15], a[16:19], v[160:163]\n"
      "s_waitcnt lgkmcnt(0)\n"
      "v_mfma_f32_16x16x32_bf16 v[64:67], a[48:51], a[64:67], v[64:67]\n"
      "s_waitcnt vmcnt(12)\n"
      "s_barrier\n"
      "v_mfma_f32_16x16x32_bf16 v[68:71], a[48:51], a[68:71], v[68:71]\n"
      "ds_read_b128 a[0:3], v45 offset:32768\n"
      "v_mfma_f32_16x16x32_bf16 v[72:75], a[48:51], a[72:75], v[72:75]\n"
      "ds_read_b128 a[16:19], v47 offset:0\n"
      "s_mov_b32 m0, s95\n"
      "v_mfma_f32_16x16x32_bf16 v[76:79], a[48:51], a[76:79], v[76:79]\n"
      "buffer_load_dwordx4 v1, s[20:23], s28 offen offset:0 lds\n"
      "v_mfma_f32_16x16x32_bf16 v[80:83], a[48:51], a[80:83], v[80:83]\n"
      "ds_read_b128 a[20:23], v47 offset:2048\n"
      "v_mfma_f32_16x16x32_bf16 v[84:87], a[48:51], a[84:87], v[84:87]\n"
      "ds_read_b128 a[24:27], v47 offset:4096\n"
      "v_mfma_f32_16x16x32_bf16 v[88:91], a[48:51], a[88:91], v[88:91]\n"
      "ds_read_b128 a[28:31], v47 offset:6144\n"
      "v_mfma_f32_16x16x32_bf16 v[92:95], a[48:51], a[92:95], v[92:95]\n"
      "ds_read_b128 a[32:35], v47 offset:8192\n"
      "v_mfma_f32_16x16x32_bf16 v[124:127], a[52:55], a[92:95], v[124:127]\n"
      "buffer_load_dwordx4 v2, s[20:23], s28 offen offset:1024 lds\n"
      "v_mfma_f32_16x16x32_bf16 v[120:123], a[52:55], a[88:91], v[120:123]\n"
      "ds_read_b128 a[36:39], v47 offset:10240\n"
      "v_mfma_f32_16x16x32_bf16 v[116:119], a[52:55], a[84:87], v[116:119]\n"
      "ds_read_b128 a[40:43], v47 offset:12288\n"
      "v_mfma_f32_16x16x32_bf16 v[112:115], a[52:55], a[80:83], v[112:115]\n"
      "ds_read_b128 a[44:47], v47 offset:14336\n"
      "v_mfma_f32_16x16x32_bf16 v[108:111], a[52:55], a[76:79], v[108:111]\n"
      "ds_read_b128 a[4:7], v45 offset:34816\n"
      "v_mfma_f32_16x16x32_bf16 v[104:107], a[52:55], a[72:75], v[104:107]\n"
      "buffer_load_dwordx4 v3, s[20:23], s28 offen offset:2048 lds\n"
      "v_mfma_f32_16x16x32_bf16 v[100:103], a[52:55], a[68:71], v[100:103]\n"
      "ds_read_b128 a[8:11], v45 offset:36864\n"
      "v_mfma_f32_16x16x32_bf16 v[96:99], a[52:55], a[64:67], v[96:99]\n"
      "ds_read_b128 a[12:15], v45 offset:38912\n"
      "v_mfma_f32_16x16x32_bf16 v[128:131], a[56:59], a[64:67], v[128:131]\n"
      "v_mfma_f32_16x16x32_bf16 v[132:135], a[56:59], a[68:71], v[132:135]\n"
      "v_mfma_f32_16x16x32_bf16 v[136:139], a[56:59], a[72:75], v[136:139]\n"
      "buffer_load_dwordx4 v63, s[20:23], s28 offen offset:3072 lds\n"
      "v_mfma_f32_16x16x32_bf16 v[140:143], a[56:59], a[76:79], v[140:143]\n"
      "s_and_b32 s83, s10, 15\n"
      "v_mfma_f32_16x16x32_bf16 v[144:147], a[56:59], a[80:83], v[144:147]\n"
      "s_lshr_b64 s[10:11], s[10:11], 4\n"
      "v_mfma_f32_16x16x32_bf16 v[148:151], a[56:59], a[84:87], v[148:151]\n"
      "s_lshl_b32 s28, s83, 17\n"
      "v_mfma_f32_16x16x32_bf16 v[152:155], a[56:59], a[88:91], v[152:155]\n"
      "s_add_u32 s28, s28, s91\n"
      "s_add_u32 m0, s96, 0\n"
      "v_mfma_f32_16x16x32_bf16 v[156:159], a[56:59], a[92:95], v[156:159]\n"
      "buffer_load_dwordx4 v23, s[64:67], s29 offen offset:0 sc1 lds\n"
      "v_mfma_f32_16x16x32_bf16 v[188:191], a[60:63], a[92:95], v[188:191]\n"
      "v_mfma_f32_16x16x32_bf16 v[184:187], a[60:63], a[88:91], v[184:187]\n"
      "v_mfma_f32_16x16x32_bf16 v[180:183], a[60:63], a[84:87], v[180:183]\n"
      "v_mfma_f32_16x16x32_bf16 v[176:179], a[60:63], a[80:83], v[176:179]\n"
      "v_mfma_f32_16x16x32_bf16 v[172:175], a[60:63], a[76:79], v[172:175]\n"
      "buffer_load_dwordx4 v23, s[64:67], s29 offen offset:1024 sc1 lds\n"
      "v_mfma_f32_16x16x32_bf16 v[168:171], a[60:63], a[72:75], v[168:171]\n"
      "v_mfma_f32_16x16x32_bf16 v[164:167], a[60:63], a[68:71], v[164:167]\n"
      "v_mfma_f32_16x16x32_bf16 v[160:163], a[60:63], a[64:67], v[160:163]\n"
      "s_waitcnt lgkmcnt(0)\n"
      "v_mfma_f32_16x16x32_bf16 v[64:67], a[0:3], a[16:19], v[64:67]\n"
      "ds_read_b128 a[48:51], v46 offset:32768\n"
      "v_mfma_f32_16x16x32_bf16 v[68:71], a[0:3], a[20:23], v[68:71]\n"
      "ds_read_b128 a[64:67], v48 offset:0\n"
      "v_mfma_f32_16x16x32_bf16 v[72:75], a[0:3], a[24:27], v[72:75]\n"
      "ds_read_b128 a[68:71], v48 offset:2048\n"
      "v_mfma_f32_16x16x32_bf16 v[76:79], a[0:3], a[28:31], v[76:79]\n"
      "buffer_load_dwordx4 v23, s[64:67], s29 offen offset:2048 sc1 lds\n"
      "v_mfma_f32_16x16x32_bf16 v[80:83], a[0:3], a[32:35], v[80:83]\n"
      "ds_read_b128 a[72:75], v48 offset:4096\n"
      "v_mfma_f32_16x16x32_bf16 v[84:87], a[0:3], a[36:39], v[84:87]\n"
      "ds_read_b128 a[76:79], v48 offset:6144\n"
      "v_mfma_f32_16x16x32_bf16 v[88:91], a[0:3], a[40:43], v[88:91]\n"
      "ds_read_b128 a[80:83], v48 offset:8192\n"
      "v_mfma_f32_16x16x32_bf16 v[92:95], a[0:3], a[44:47], v[92:95]\n"
      "ds_read_b128 a[84:87], v48 offset:10240\n"
      "v_mfma_f32_16x16x32_bf16 v[124:127], a[4:7], a[44:47], v[124:127]\n"
      "buffer_load_dwordx4 v23, s[64:67], s29 offen offset:3072 sc1 lds\n"
      "v_mfma_f32_16x16x32_bf16 v[120:123], a[4:7], a[40:43], v[120:123]\n"
      "ds_read_b128 a[88:91], v48 offset:12288\n"
      "v_mfma_f32_16x16x32_bf16 v[116:119], a[4:7], a[36:39], v[116:119]\n"
      "ds_read_b128 a[92:95], v48 offset:14336\n"
      "v_mfma_f32_16x16x32_bf16 v[112:115], a[4:7], a[32:35], v[112:115]\n"
      "ds_read_b128 a[52:55], v46 offset:34816\n"
      "v_mfma_f32_16x16x32_bf16 v[108:111], a[4:7], a[28:31], v[108:111]\n"
      "ds_read_b128 a[56:59], v46 offset:36864\n"
      "s_add_u32 m0, s96, 4096\n"
      "v_mfma_f32_16x16x32_bf16 v[104:107], a[4:7], a[24:27], v[104:107]\n"
      "buffer_load_dwordx4 v24, s[64:67], s29 offen offset:0 sc1 lds\n"
      "v_mfma_f32_16x16x32_bf16 v[100:103], a[4:7], a[20:23], v[100:103]\n"
      "ds_read_b128 a[60:63], v46 offset:38912\n"
      "v_mfma_f32_16x16x32_bf16 v[96:99], a[4:7], a[16:19], v[96:99]\n"
      "s_add_u32 s96, s94, s85\n"
      "v_mfma_f32_16x16x32_bf16 v[128:131], a[8:11], a[16:19], v[128:131]\n"
      "s_add_u32 s95, s94, s92\n"
      "s_add_u32 s95, s95, 32768\n"
      "v_mfma_f32_16x16x32_bf16 v[132:135], a[8:11], a[20:23], v[132:135]\n"
      "s_cmp_eq_u32 s94, 98304\n"
      "s_cselect_b32 s97, 0xfffe8000, s99\n"
      "s_add_u32 s94, s94, s97\n"
      "v_mfma_f32_16x16x32_bf16 v[136:139], a[8:11], a[24:27], v[136:139]\n"
      "buffer_load_dwordx4 v24, s[64:67], s29 offen offset:1024 sc1 lds\n"
      "v_mfma_f32_16x16x32_bf16 v[140:143], a[8:11], a[28:31], v[140:143]\n"
      "v_add_u32 v49, s94, v49\n"
      "v_add_u32 v50, s94, v50\n"
      "v_mfma_f32_16x16x32_bf16 v[144:147], a[8:11], a[32:35], v[144:147]\n"
      "v_add_u32 v51, s94, v51\n"
      "v_add_u32 v52, s94, v52\n"
      "v_mfma_f32_16x16x32_bf16 v[148:151], a[8:11], a[36:39], v[148:151]\n"
      "v_add_u32 v53, s94, v53\n"
      "v_add_u32 v54, s94, v54\n"
      "v_mfma_f32_16x16x32_bf16 v[152:155], a[8:11], a[40:43], v[152:155]\n"
      "v_mfma_f32_16x16x32_bf16 v[156:159], a[8:11], a[44:47], v[156:159]\n"
      "buffer_load_dwordx4 v24, s[64:67], s29 offen offset:2048 sc1 lds\n"
      "v_mfma_f32_16x16x32_bf16 v[188:191], a[12:15], a[44:47], v[188:191]\n"
      "v_mfma_f32_16x16x32_bf16 v[184:187], a[12:15], a[40:43], v[184:187]\n"
      "v_mfma_f32_16x16x32_bf16 v[180:183], a[12:15], a[36:39], v[180:183]\n"
      "v_mfma_f32_16x16x32_bf16 v[176:179], a[12:15], a[32:35], v[176:179]\n"
      "v_mfma_f32_16x16x32_bf16 v[172:175], a[12:15], a[28:31], v[172:175]\n"
      "buffer_load_dwordx4 v24, s[64:67], s29 offen offset:3072 sc1 lds\n"
      "v_mfma_f32_16x16x32_bf16 v[168:171], a[12:15], a[24:27], v[168:171]\n"
      "s_lshl_b32 s29, s83, 15\n"
      "s_add_u32 s29, s29, s98\n"
      "v_mfma_f32_16x16x32_bf16 v[164:167], a[12:15], a[20:23], v[164:167]\n"
      "v_mfma_f32_16x16x32_bf16 v[160:163], a[12:15], a[16:19], v[160:163]\n"
      "Lk_nn:\n"
      "s_waitcnt lgkmcnt(0)\n"
      "v_mfma_f32_16x16x32_bf16 v[64:67], a[48:51], a[64:67], v[64:67]\n"
      "s_waitcnt vmcnt(12)\n"
      "s_barrier\n"
      "v_mfma_f32_16x16x32_bf16 v[68:71], a[48:51], a[68:71], v[68:71]\n"
      "ds_read_b64_tr_b16 a[0:1], v49 offset:32768\n"
      "v_mfma_f32_16x16x32_bf16 v[72:75], a[48:51], a[72:75], v[72:75]\n"
      "ds_read_b64_tr_b16 a[2:3], v49 offset:33792\n"
      "s_mov_b32 m0, s95\n"
      "v_mfma_f32_16x16x32_bf16 v[76:79], a[48:51], a[76:79], v[76:79]\n"
      "buffer_load_dwordx4 v1, s[20:23], s28 offen offset:0 lds\n"
      "v_mfma_f32_16x16x32_bf16 v[80:83], a[48:51], a[80:83], v[80:83]\n"
      "ds_read_b128 a[16:19], v53 offset:0\n"
      "v_mfma_f32_16x16x32_bf16 v[84:87], a[48:51], a[84:87], v[84:87]\n"
      "ds_read_b128 a[20:23], v53 offset:2048\n"
      "v_mfma_f32_16x16x32_bf16 v[88:91], a[48:51], a[88:91], v[88:91]\n"
      "ds_read_b128 a[24:27], v53 offset:4096\n"
      "v_mfma_f32_16x16x32_bf16 v[92:95], a[48:51], a[92:95], v[92:95]\n"
      "ds_read_b128 a[28:31], v53 offset:6144\n"
      "v_mfma_f32_16x16x32_bf16 v[124:127], a[52:55], a[92:95], v[124:127]\n"
      "buffer_load_dwordx4 v2, s[20:23], s28 offen offset:1024 lds\n"
      "v_mfma_f32_16x16x32_bf16 v[120:123], a[52:55], a[88:91], v[120:123]\n"
      "ds_read_b128 a[32:35], v53 offset:8192\n"
      "v_mfma_f32_16x16x32_bf16 v[116:119], a[52:55], a[84:87], v[116:119]\n"
      "ds_read_b128 a[36:39], v53 offset:10240\n"
      "v_mfma_f32_16x16x32_bf16 v[112:115], a[52:55], a[80:83], v[112:115]\n"
      "ds_read_b128 a[40:43], v53 offset:12288\n"
      "v_mfma_f32_16x16x32_bf16 v[108:111], a[52:55], a[76:79], v[108:111]\n"
      "ds_read_b128 a[44:47], v53 offset:14336\n"
      "v_mfma_f32_16x16x32_bf16 v[104:107], a[52:55], a[72:75], v[104:107]\n"
      "buffer_load_dwordx4 v3, s[20:23], s28 offen offset:2048 lds\n"
      "v_mfma_f32_16x16x32_bf16 v[100:103], a[52:55], a[68:71], v[100:103]\n"
      "ds_read_b64_tr_b16 a[4:5], v50 offset:32768\n"
      "v_mfma_f32_16x16x32_bf16 v[96:99], a[52:55], a[64:67], v[96:99]\n"
      "ds_read_b64_tr_b16 a[6:7], v50 offset:33792\n"
      "v_mfma_f32_16x16x32_bf16 v[128:131], a[56:59], a[64:67], v[128:131]\n"
      "ds_read_b64_tr_b16 a[8:9], v51 offset:32768\n"
      "v_mfma_f32_16x16x32_bf16 v[132:135], a[56:59], a[68:71], v[132:135]\n"
      "ds_read_b64_tr_b16 a[10:11], v51 offset:33792\n"
      "v_mfma_f32_16x16x32_bf16 v[136:139], a[56:59], a[72:75], v[136:139]\n"
      "buffer_load_dwordx4 v63, s[20:23], s28 offen offset:3072 lds\n"
      "v_mfma_f32_16x16x32_bf16 v[140:143], a[56:59], a[76:79], v[140:143]\n"
      "ds_read_b64_tr_b16 a[12:13], v52 offset:32768\n"
      "s_and_b32 s83, s10, 15\n"
      "v_mfma_f32_16x16x32_bf16 v[144:147], a[56:59], a[80:83], v[144:147]\n"
      "ds_read_b64_tr_b16 a[14:15], v52 offset:33792\n"
      "s_lshr_b64 s[10:11], s[10:11], 4\n"
      "v_mfma_f32_16x16x32_bf16 v[148:151], a[56:59], a[84:87], v[148:151]\n"
      "s_lshl_b32 s28, s83, 17\n"
      "v_mfma_f32_16x16x32_bf16 v[152:155], a[56:59], a[88:91], v[152:155]\n"
      "s_add_u32 s28, s28, s91\n"
      "s_add_u32 m0, s96, 0\n"
      "v_mfma_f32_16x16x32_bf16 v[156:159], a[56:59], a[92:95], v[156:159]\n"
      "buffer_load_dwordx4 v23, s[64:67], s29 offen offset:0 sc1 lds\n"
      "v_mfma_f32_16x16x32_bf16 v[188:191], a[60:63], a[92:95], v[188:191]\n"
      "v_mfma_f32_16x16x32_bf16 v[184:187], a[60:63], a[88:91], v[184:187]\n"
      "v_mfma_f32_16x16x32_bf16 v[180:183], a[60:63], a[84:87], v[180:183]\n"
      "v_mfma_f32_16x16x32_bf16 v[176:179], a[60:63], a[80:83], v[176:179]\n"
      "v_mfma_f32_16x16x32_bf16 v[172:175], a[60:63], a[76:79], v[172:175]\n"
      "buffer_load_dwordx4 v23, s[64:67], s29 offen offset:1024 sc1 lds\n"
      "v_mfma_f32_16x16x32_bf16 v[168:171], a[60:63], a[72:75], v[168:171]\n"
      "v_mfma_f32_16x16x32_bf16 v[164:167], a[60:63], a[68:71], v[164:167]\n"
      "v_mfma_f32_16x16x32_bf16 v[160:163], a[60:63], a[64:67], v[160:163]\n"
      "s_waitcnt lgkmcnt(0)\n"
      "v_mfma_f32_16x16x32_bf16 v[64:67], a[0:3], a[16:19], v[64:67]\n"
      "ds_read_b64_tr_b16 a[48:49], v49 offset:40960\n"
      "v_mfma_f32_16x16x32_bf16 v[68:71], a[0:3], a[20:23], v[68:71]\n"
      "ds_read_b64_tr_b16 a[50:51], v49 offset:41984\n"
      "v_mfma_f32_16x16x32_bf16 v[72:75], a[0:3], a[24:27], v[72:75]\n"
      "ds_read_b128 a[64:67], v54 offset:0\n"
      "v_mfma_f32_16x16x32_bf16 v[76:79], a[0:3], a[28:31], v[76:79]\n"
      "buffer_load_dwordx4 v23, s[64:67], s29 offen offset:2048 sc1 lds\n"
      "v_mfma_f32_16x16x32_bf16 v[80:83], a[0:3], a[32:35], v[80:83]\n"
      "ds_read_b128 a[68:71], v54 offset:2048\n"
      "v_mfma_f32_16x16x32_bf16 v[84:87], a[0:3], a[36:39], v[84:87]\n"
      "ds_read_b128 a[72:75], v54 offset:4096\n"
      "v_mfma_f32_16x16x32_bf16 v[88:91], a[0:3], a[40:43], v[88:91]\n"
      "ds_read_b128 a[76:79], v54 offset:6144\n"
      "v_mfma_f32_16x16x32_bf16 v[92:95], a[0:3], a[44:47], v[92:95]\n"
      "ds_read_b128 a[80:83], v54 offset:8192\n"
      "v_mfma_f32_16x16x32_bf16 v[124:127], a[4:7], a[44:47], v[124:127]\n"
      "buffer_load_dwordx4 v23, s[64:67], s29 offen offset:3072 sc1 lds\n"
      "v_mfma_f32_16x16x32_bf16 v[120:123], a[4:7], a[40:43], v[120:123]\n"
      "ds_read_b128 a[84:87], v54 offset:10240\n"
      "v_mfma_f32_16x16x32_bf16 v[116:119], a[4:7], a[36:39], v[116:119]\n"
      "ds_read_b128 a[88:91], v54 offset:12288\n"
      "v_mfma_f32_16x16x32_bf16 v[112:115], a[4:7], a[32:35], v[112:115]\n"
      "ds_read_b128 a[92:95], v54 offset:14336\n"
      "v_mfma_f32_16x16x32_bf16 v[108:111], a[4:7], a[28:31], v[108:111]\n"
      "ds_read_b64_tr_b16 a[52:53], v50 offset:40960\n"
      "s_add_u32 m0, s96, 4096\n"
      "v_mfma_f32_16x16x32_bf16 v[104:107], a[4:7], a[24:27], v[104:107]\n"
      "buffer_load_dwordx4 v24, s[64:67], s29 offen offset:0 sc1 lds\n"
      "v_mfma_f32_16x16x32_bf16 v[100:103], a[4:7], a[20:23], v[100:103]\n"
      "ds_read_b64_tr_b16 a[54:55], v50 offset:41984\n"
      "v_mfma_f32_16x16x32_bf16 v[96:99], a[4:7], a[16:19], v[96:99]\n"
      "ds_read_b64_tr_b16 a[56:57], v51 offset:40960\n"
      "v_mfma_f32_16x16x32_bf16 v[128:131], a[8:11], a[16:19], v[128:131]\n"
      "ds_read_b64_tr_b16 a[58:59], v51 offset:41984\n"
      "v_mfma_f32_16x16x32_bf16 v[132:135], a[8:11], a[20:23], v[132:135]\n"
      "ds_read_b64_tr_b16 a[60:61], v52 offset:40960\n"
      "v_mfma_f32_16x16x32_bf16 v[136:139], a[8:11], a[24:27], v[136:139]\n"
      "buffer_load_dwordx4 v24, s[64:67], s29 offen offset:1024 sc1 lds\n"
      "v_mfma_f32_16x16x32_bf16 v[140:143], a[8:11], a[28:31], v[140:143]\n"
      "ds_read_b64_tr_b16 a[62:63], v52 offset:41984\n"
      "v_mfma_f32_16x16x32_bf16 v[144:147], a[8:11], a[32:35], v[144:147]\n"
      "s_add_u32 s96, s94, s85\n"
      "v_mfma_f32_16x16x32_bf16 v[148:151], a[8:11], a[36:39], v[148:151]\n"
      "s_add_u32 s95, s94, s92\n"
      "s_add_u32 s95, s95, 32768\n"
      "v_mfma_f32_16x16x32_bf16 v[152:155], a[8:11], a[40:43], v[152:155]\n"
      "s_cmp_eq_u32 s94, 98304\n"
      "s_cselect_b32 s97, 0xfffe8000, s99\n"
      "s_add_u32 s94, s94, s97\n"
      "v_mfma_f32_16x16x32_bf16 v[156:159], a[8:11], a[44:47], v[156:159]\n"
      "buffer_load_dwordx4 v24, s[64:67], s29 offen offset:2048 sc1 lds\n"
      "v_mfma_f32_16x16x32_bf16 v[188:191], a[12:15], a[44:47], v[188:191]\n"
      "v_add_u32 v49, s97, v49\n"
      "v_add_u32 v50, s97, v50\n"
      "v_mfma_f32_16x16x32_bf16 v[184:187], a[12:15], a[40:43], v[184:187]\n"
      "v_add_u32 v51, s97, v51\n"
      "v_add_u32 v52, s97, v52\n"
      "v_mfma_f32_16x16x32_bf16 v[180:183], a[12:15], a[36:39], v[180:183]\n"
      "v_add_u32 v53, s97, v53\n"
      "v_add_u32 v54, s97, v54\n"
      "v_mfma_f32_16x16x32_bf16 v[176:179], a[12:15], a[32:35], v[176:179]\n"
      "v_mfma_f32_16x16x32_bf16 v[172:175], a[12:15], a[28:31], v[172:175]\n"
      "buffer_load_dwordx4 v24, s[64:67], s29 offen offset:3072 sc1 lds\n"
      "v_mfma_f32_16x16x32_bf16 v[168:171], a[12:15], a[24:27], v[168:171]\n"
      "s_lshl_b32 s29, s83, 15\n"
      "s_add_u32 s29, s29, s98\n"
      "v_mfma_f32_16x16x32_bf16 v[164:167], a[12:15], a[20:23], v[164:167]\n"
      "v_mfma_f32_16x16x32_bf16 v[160:163], a[12:15], a[16:19], v[160:163]\n"
      "s_sub_u32 s93, s93, 1\n"
      "s_cmp_lg_u32 s93, 0\n"
      "s_cbranch_scc1 Lk_nn\n"
      "Lk_nn_x:\n"
      "Lk_xchg:\n"
      "v_mov_b32 v16, v53\n"
      "v_mov_b32 v17, v54\n"
      "s_waitcnt vmcnt(12) lgkmcnt(0)\n"
      "s_barrier\n"
      "v_mfma_f32_16x16x32_bf16 v[64:67], a[48:51], a[64:67], v[64:67]\n"
      "ds_read_b64_tr_b16 a[96:97], v49 offset:32768\n"
      "s_mov_b32 m0, s95\n"
      "v_mfma_f32_16x16x32_bf16 v[68:71], a[48:51], a[68:71], v[68:71]\n"
      "buffer_load_dwordx4 v1, s[20:23], s28 offen offset:0 lds\n"
      "s_lshl_b32 s72, s57, 3\n"
      "v_mfma_f32_16x16x32_bf16 v[72:75], a[48:51], a[72:75], v[72:75]\n"
      "ds_read_b64_tr_b16 a[98:99], v49 offset:33792\n"
      "s_add_u32 s72, s72, s58\n"
      "v_mfma_f32_16x16x32_bf16 v[76:79], a[48:51], a[76:79], v[76:79]\n"
      "buffer_load_dwordx4 v2, s[20:23], s28 offen offset:1024 lds\n"
      "s_lshl_b32 s80, s72, 2\n"
      "v_mfma_f32_16x16x32_bf16 v[80:83], a[48:51], a[80:83], v[80:83]\n"
      "ds_read_b128 a[0:3], v16 offset:8192\n"
      "s_add_u32 s80, s80, s61\n"
      "v_mfma_f32_16x16x32_bf16 v[84:87], a[48:51], a[84:87], v[84:87]\n"
      "s_lshl_b32 s80, s80, 9\n"
      "s_add_u32 s80, s80, 262144\n"
      "v_mfma_f32_16x16x32_bf16 v[88:91], a[48:51], a[88:91], v[88:91]\n"
      "ds_read_b128 a[4:7], v16 offset:10240\n"
      "buffer_load_dwordx4 v3, s[20:23], s28 offen offset:2048 lds\n"
      "v_mfma_f32_16x16x32_bf16 v[92:95], a[48:51], a[92:95], v[92:95]\n"
      "s_lshl_b32 s72, s72, 18\n"
      "s_add_u32 s72, s72, s92\n"
      "v_mfma_f32_16x16x32_bf16 v[124:127], a[52:55], a[92:95], v[124:127]\n"
      "ds_read_b128 a[8:11], v16 offset:12288\n"
      "buffer_load_dwordx4 v63, s[20:23], s28 offen offset:3072 lds\n"
      "v_mfma_f32_16x16x32_bf16 v[120:123], a[52:55], a[88:91], v[120:123]\n"
      "s_add_u32 s72, s72, 0x2000000\n"
      "s_xor_b32 s73, s56, 1\n"
      "v_mfma_f32_16x16x32_bf16 v[116:119], a[52:55], a[84:87], v[116:119]\n"
      "ds_read_b128 a[12:15], v16 offset:14336\n"
      "s_add_u32 m0, s96, 0\n"
      "v_mfma_f32_16x16x32_bf16 v[112:115], a[52:55], a[80:83], v[112:115]\n"
      "buffer_load_dwordx4 v23, s[64:67], s29 offen offset:0 sc1 lds\n"
      "s_lshl_b32 s73, s73, 16\n"
      "v_mfma_f32_16x16x32_bf16 v[108:111], a[52:55], a[76:79], v[108:111]\n"
      "ds_read_b64_tr_b16 a[100:101], v50 offset:32768\n"
      "s_add_u32 s73, s73, s72\n"
      "v_mfma_f32_16x16x32_bf16 v[104:107], a[52:55], a[72:75], v[104:107]\n"
      "buffer_load_dwordx4 v23, s[64:67], s29 offen offset:1024 sc1 lds\n"
      "s_add_u32 s73, s73, 16384\n"
      "v_mfma_f32_16x16x32_bf16 v[100:103], a[52:55], a[68:71], v[100:103]\n"
      "ds_read_b64_tr_b16 a[102:103], v50 offset:33792\n"
      "s_xor_b32 s74, s56, 2\n"
      "v_mfma_f32_16x16x32_bf16 v[96:99], a[52:55], a[64:67], v[96:99]\n"
      "s_lshl_b32 s74, s74, 16\n"
      "s_add_u32 s74, s74, s72\n"
      "v_mfma_f32_16x16x32_bf16 v[128:131], a[56:59], a[64:67], v[128:131]\n"
      "ds_read_b64_tr_b16 a[104:105], v51 offset:32768\n"
      "buffer_load_dwordx4 v23, s[64:67], s29 offen offset:2048 sc1 lds\n"
      "v_mfma_f32_16x16x32_bf16 v[132:135], a[56:59], a[68:71], v[132:135]\n"
      "s_add_u32 s74, s74, 32768\n"
      "s_xor_b32 s75, s56, 3\n"
      "v_mfma_f32_16x16x32_bf16 v[136:139], a[56:59], a[72:75], v[136:139]\n"
      "ds_read_b64_tr_b16 a[106:107], v51 offset:33792\n"
      "buffer_load_dwordx4 v23, s[64:67], s29 offen offset:3072 sc1 lds\n"
      "v_mfma_f32_16x16x32_bf16 v[140:143], a[56:59], a[76:79], v[140:143]\n"
      "s_lshl_b32 s75, s75, 16\n"
      "s_add_u32 s75, s75, s72\n"
      "v_mfma_f32_16x16x32_bf16 v[144:147], a[56:59], a[80:83], v[144:147]\n"
      "ds_read_b64_tr_b16 a[108:109], v52 offset:32768\n"
      "s_add_u32 m0, s96, 4096\n"
      "v_mfma_f32_16x16x32_bf16 v[148:151], a[56:59], a[84:87], v[148:151]\n"
      "buffer_load_dwordx4 v24, s[64:67], s29 offen offset:0 sc1 lds\n"
      "s_add_u32 s75, s75, 49152\n"
      "v_mfma_f32_16x16x32_bf16 v[152:155], a[56:59], a[88:91], v[152:155]\n"
      "ds_read_b64_tr_b16 a[110:111], v52 offset:33792\n"
      "s_lshl_b32 s76, s56, 16\n"
      "v_mfma_f32_16x16x32_bf16 v[156:159], a[56:59], a[92:95], v[156:159]\n"
      "buffer_load_dwordx4 v24, s[64:67], s29 offen offset:1024 sc1 lds\n"
      "s_add_u32 s76, s76, s72\n"
      "v_mfma_f32_16x16x32_bf16 v[188:191], a[60:63], a[92:95], v[188:191]\n"
      "s_lshl_b32 s77, s57, 1\n"
      "s_add_u32 s77, s77, s63\n"
      "v_mfma_f32_16x16x32_bf16 v[184:187], a[60:63], a[88:91], v[184:187]\n"
      "s_lshl_b32 s77, s77, 2\n"
      "s_add_u32 s77, s77, s56\n"
      "v_mfma_f32_16x16x32_bf16 v[180:183], a[60:63], a[84:87], v[180:183]\n"
      "buffer_load_dwordx4 v24, s[64:67], s29 offen offset:2048 sc1 lds\n"
      "s_lshl_b32 s77, s77, 16\n"
      "v_mfma_f32_16x16x32_bf16 v[176:179], a[60:63], a[80:83], v[176:179]\n"
      "s_lshl_b32 s78, s58, 1\n"
      "s_add_u32 s78, s78, s62\n"
      "v_mfma_f32_16x16x32_bf16 v[172:175], a[60:63], a[76:79], v[172:175]\n"
      "buffer_load_dwordx4 v24, s[64:67], s29 offen offset:3072 sc1 lds\n"
      "s_lshl_b32 s78, s78, 7\n"
      "v_mfma_f32_16x16x32_bf16 v[168:171], a[60:63], a[72:75], v[168:171]\n"
      "s_add_u32 s77, s77, s78\n"
      "s_add_u32 s78, s77, 32768\n"
      "v_mfma_f32_16x16x32_bf16 v[164:167], a[60:63], a[68:71], v[164:167]\n"
      "v_and_b32 v5, 15, v4\n"
      "v_lshlrev_b32 v5, 11, v5\n"
      "v_mfma_f32_16x16x32_bf16 v[160:163], a[60:63], a[64:67], v[160:163]\n"
      "v_bfe_u32 v6, v4, 4, 1\n"
      "v_bfe_u32 v7, v4, 5, 1\n"
      "s_waitcnt lgkmcnt(0)\n"
      "v_mfma_f32_16x16x32_bf16 v[80:83], a[96:99], a[0:3], v[80:83]\n"
      "ds_read_b64_tr_b16 a[112:113], v49 offset:40960\n"
      "v_mfma_f32_16x16x32_bf16 v[84:87], a[96:99], a[4:7], v[84:87]\n"
      "ds_read_b64_tr_b16 a[114:115], v49 offset:41984\n"
      "v_mfma_f32_16x16x32_bf16 v[88:91], a[96:99], a[8:11], v[88:91]\n"
      "ds_read_b128 a[16:19], v17 offset:8192\n"
      "v_mfma_f32_16x16x32_bf16 v[92:95], a[96:99], a[12:15], v[92:95]\n"
      "ds_read_b128 a[20:23], v17 offset:10240\n"
      "v_mfma_f32_16x16x32_bf16 v[124:127], a[100:103], a[12:15], v[124:127]\n"
      "ds_read_b128 a[24:27], v17 offset:12288\n"
      "v_mfma_f32_16x16x32_bf16 v[120:123], a[100:103], a[8:11], v[120:123]\n"
      "ds_read_b128 a[28:31], v17 offset:14336\n"
      "v_mfma_f32_16x16x32_bf16 v[116:119], a[100:103], a[4:7], v[116:119]\n"
      "ds_read_b64_tr_b16 a[116:117], v50 offset:40960\n"
      "v_mfma_f32_16x16x32_bf16 v[112:115], a[100:103], a[0:3], v[112:115]\n"
      "ds_read_b64_tr_b16 a[118:119], v50 offset:41984\n"
      "v_mfma_f32_16x16x32_bf16 v[144:147], a[104:107], a[0:3], v[144:147]\n"
      "ds_read_b64_tr_b16 a[120:121], v51 offset:40960\n"
      "v_mfma_f32_16x16x32_bf16 v[148:151], a[104:107], a[4:7], v[148:151]\n"
      "ds_read_b64_tr_b16 a[122:123], v51 offset:41984\n"
      "v_mfma_f32_16x16x32_bf16 v[152:155], a[104:107], a[8:11], v[152:155]\n"
      "ds_read_b64_tr_b16 a[124:125], v52 offset:40960\n"
      "v_mfma_f32_16x16x32_bf16 v[156:159], a[104:107], a[12:15], v[156:159]\n"
      "ds_read_b64_tr_b16 a[126:127], v52 offset:41984\n"
      "v_mfma_f32_16x16x32_bf16 v[188:191], a[108:111], a[12:15], v[188:191]\n"
      "s_cmp_eq_u32 s94, 98304\n"
      "s_cselect_b32 s97, 0xfffe8000, s99\n"
      "s_add_u32 s94, s94, s97\n"
      "v_mfma_f32_16x16x32_bf16 v[184:187], a[108:111], a[8:11], v[184:187]\n"
      "v_add_u32 v49, s97, v49\n"
      "v_add_u32 v50, s97, v50\n"
      "v_add_u32 v51, s97, v51\n"
      "v_mfma_f32_16x16x32_bf16 v[180:183], a[108:111], a[4:7], v[180:183]\n"
      "v_add_u32 v52, s97, v52\n"
      "v_add_u32 v18, s97, v16\n"
      "v_add_u32 v19, s97, v17\n"
      "v_mfma_f32_16x16x32_bf16 v[176:179], a[108:111], a[0:3], v[176:179]\n"
      "s_waitcnt vmcnt(12) lgkmcnt(0)\n"
      "s_barrier\n"
      "v_mfma_f32_16x16x32_bf16 v[80:83], a[112:115], a[16:19], v[80:83]\n"
      "ds_read_b64_tr_b16 a[128:129], v49 offset:32768\n"
      "v_lshl_add_u32 v5, v6, 5, v5\n"
      "v_mfma_f32_16x16x32_bf16 v[84:87], a[112:115], a[20:23], v[84:87]\n"
      "ds_read_b64_tr_b16 a[130:131], v49 offset:33792\n"
      "v_lshl_add_u32 v59, v7, 4, v5\n"
      "v_mfma_f32_16x16x32_bf16 v[88:91], a[112:115], a[24:27], v[88:91]\n"
      "ds_read_b128 a[0:3], v18 offset:8192\n"
      "v_lshlrev_b32 v58, 4, v4\n"
      "v_mfma_f32_16x16x32_bf16 v[92:95], a[112:115], a[28:31], v[92:95]\n"
      "ds_read_b128 a[4:7], v18 offset:10240\n"
      "s_mov_b32 s24, s50\n"
      "v_mfma_f32_16x16x32_bf16 v[124:127], a[116:119], a[28:31], v[124:127]\n"
      "ds_read_b128 a[8:11], v18 offset:12288\n"
      "s_and_b32 s25, s51, 0xffff\n"
      "v_mfma_f32_16x16x32_bf16 v[120:123], a[116:119], a[24:27], v[120:123]\n"
      "ds_read_b128 a[12:15], v18 offset:14336\n"
      "s_mov_b32 s26, 0x400000\n"
      "v_mfma_f32_16x16x32_bf16 v[116:119], a[116:119], a[20:23], v[116:119]\n"
      "ds_read_b64_tr_b16 a[132:133], v50 offset:32768\n"
      "s_mov_b32 s27, 0x20000\n"
      "v_mfma_f32_16x16x32_bf16 v[112:115], a[116:119], a[16:19], v[112:115]\n"
      "ds_read_b64_tr_b16 a[134:135], v50 offset:33792\n"
      "s_lshl_b32 s79, s56, 7\n"
      "v_mfma_f32_16x16x32_bf16 v[144:147], a[120:123], a[16:19], v[144:147]\n"
      "ds_read_b64_tr_b16 a[136:137], v51 offset:32768\n"
      "s_add_u32 s79, s79, s80\n"
      "v_mfma_f32_16x16x32_bf16 v[148:151], a[120:123], a[20:23], v[148:151]\n"
      "ds_read_b64_tr_b16 a[138:139], v51 offset:33792\n"
      "v_min_u32 v55, 2, v4\n"
      "v_mfma_f32_16x16x32_bf16 v[152:155], a[120:123], a[24:27], v[152:155]\n"
      "ds_read_b64_tr_b16 a[140:141], v52 offset:32768\n"
      "v_add_u32 v55, 1, v55\n"
      "v_mfma_f32_16x16x32_bf16 v[156:159], a[120:123], a[28:31], v[156:159]\n"
      "ds_read_b64_tr_b16 a[142:143], v52 offset:33792\n"
      "v_xor_b32 v55, s56, v55\n"
      "v_mfma_f32_16x16x32_bf16 v[188:191], a[124:127], a[28:31], v[188:191]\n"
      "v_lshlrev_b32 v55, 7, v55\n"
      "v_min_u32 v5, 1, v4\n"
      "v_mfma_f32_16x16x32_bf16 v[184:187], a[124:127], a[24:27], v[184:187]\n"
      "v_sub_u32 v5, 1, v5\n"
      "v_lshl_add_u32 v55, v5, 17, v55\n"
      "v_mfma_f32_16x16x32_bf16 v[180:183], a[124:127], a[20:23], v[180:183]\n"
      "v_mov_b32 v6, s54\n"
      "v_mov_b32 v7, s55\n"
      "v_mfma_f32_16x16x32_bf16 v[176:179], a[124:127], a[16:19], v[176:179]\n"
      "v_min_u32 v5, 1, v4\n"
      "v_lshlrev_b32 v5, 30, v5\n"
      "s_waitcnt lgkmcnt(0)\n"
      "v_mfma_f32_16x16x32_bf16 v[80:83], a[128:131], a[0:3], v[80:83]\n"
      "ds_read_b64_tr_b16 a[144:145], v49 offset:40960\n"
      "v_mfma_f32_16x16x32_bf16 v[84:87], a[128:131], a[4:7], v[84:87]\n"
      "ds_read_b64_tr_b16 a[146:147], v49 offset:41984\n"
      "v_mfma_f32_16x16x32_bf16 v[88:91], a[128:131], a[8:11], v[88:91]\n"
      "ds_read_b128 a[16:19], v19 offset:8192\n"
      "v_mfma_f32_16x16x32_bf16 v[92:95], a[128:131], a[12:15], v[92:95]\n"
      "ds_read_b128 a[20:23], v19 offset:10240\n"
      "v_mfma_f32_16x16x32_bf16 v[124:127], a[132:135], a[12:15], v[124:127]\n"
      "ds_read_b128 a[24:27], v19 offset:12288\n"
      "v_mfma_f32_16x16x32_bf16 v[120:123], a[132:135], a[8:11], v[120:123]\n"
      "ds_read_b128 a[28:31], v19 offset:14336\n"
      "v_mfma_f32_16x16x32_bf16 v[116:119], a[132:135], a[4:7], v[116:119]\n"
      "ds_read_b64_tr_b16 a[148:149], v50 offset:40960\n"
      "v_mfma_f32_16x16x32_bf16 v[112:115], a[132:135], a[0:3], v[112:115]\n"
      "ds_read_b64_tr_b16 a[150:151], v50 offset:41984\n"
      "v_mfma_f32_16x16x32_bf16 v[144:147], a[136:139], a[0:3], v[144:147]\n"
      "ds_read_b64_tr_b16 a[152:153], v51 offset:40960\n"
      "v_mfma_f32_16x16x32_bf16 v[148:151], a[136:139], a[4:7], v[148:151]\n"
      "ds_read_b64_tr_b16 a[154:155], v51 offset:41984\n"
      "v_mfma_f32_16x16x32_bf16 v[152:155], a[136:139], a[8:11], v[152:155]\n"
      "ds_read_b64_tr_b16 a[156:157], v52 offset:40960\n"
      "v_mfma_f32_16x16x32_bf16 v[156:159], a[136:139], a[12:15], v[156:159]\n"
      "ds_read_b64_tr_b16 a[158:159], v52 offset:41984\n"
      "v_mfma_f32_16x16x32_bf16 v[188:191], a[140:143], a[12:15], v[188:191]\n"
      "s_cmp_eq_u32 s94, 98304\n"
      "s_cselect_b32 s97, 0xfffe8000, s99\n"
      "s_add_u32 s94, s94, s97\n"
      "v_mfma_f32_16x16x32_bf16 v[184:187], a[140:143], a[8:11], v[184:187]\n"
      "v_add_u32 v49, s97, v49\n"
      "v_add_u32 v50, s97, v50\n"
      "v_add_u32 v51, s97, v51\n"
      "v_mfma_f32_16x16x32_bf16 v[180:183], a[140:143], a[4:7], v[180:183]\n"
      "v_add_u32 v52, s97, v52\n"
      "v_add_u32 v20, s97, v18\n"
      "v_add_u32 v21, s97, v19\n"
      "v_mfma_f32_16x16x32_bf16 v[176:179], a[140:143], a[0:3], v[176:179]\n"
      "s_waitcnt vmcnt(0) lgkmcnt(0)\n"
      "s_barrier\n"
      "v_mfma_f32_16x16x32_bf16 v[80:83], a[144:147], a[16:19], v[80:83]\n"
      "ds_read_b64_tr_b16 a[160:161], v49 offset:32768\n"
      "v_mfma_f32_16x16x32_bf16 v[84:87], a[144:147], a[20:23], v[84:87]\n"
      "ds_read_b64_tr_b16 a[162:163], v49 offset:33792\n"
      "v_mfma_f32_16x16x32_bf16 v[88:91], a[144:147], a[24:27], v[88:91]\n"
      "ds_read_b128 a[0:3], v20 offset:8192\n"
      "v_mfma_f32_16x16x32_bf16 v[92:95], a[144:147], a[28:31], v[92:95]\n"
      "ds_read_b128 a[4:7], v20 offset:10240\n"
      "v_mfma_f32_16x16x32_bf16 v[124:127], a[148:151], a[28:31], v[124:127]\n"
      "ds_read_b128 a[8:11], v20 offset:12288\n"
      "v_mfma_f32_16x16x32_bf16 v[120:123], a[148:151], a[24:27], v[120:123]\n"
      "ds_read_b128 a[12:15], v20 offset:14336\n"
      "v_mfma_f32_16x16x32_bf16 v[116:119], a[148:151], a[20:23], v[116:119]\n"
      "ds_read_b64_tr_b16 a[164:165], v50 offset:32768\n"
      "v_mfma_f32_16x16x32_bf16 v[112:115], a[148:151], a[16:19], v[112:115]\n"
      "ds_read_b64_tr_b16 a[166:167], v50 offset:33792\n"
      "v_mfma_f32_16x16x32_bf16 v[144:147], a[152:155], a[16:19], v[144:147]\n"
      "ds_read_b64_tr_b16 a[168:169], v51 offset:32768\n"
      "v_mfma_f32_16x16x32_bf16 v[148:151], a[152:155], a[20:23], v[148:151]\n"
      "ds_read_b64_tr_b16 a[170:171], v51 offset:33792\n"
      "v_mfma_f32_16x16x32_bf16 v[152:155], a[152:155], a[24:27], v[152:155]\n"
      "ds_read_b64_tr_b16 a[172:173], v52 offset:32768\n"
      "v_mfma_f32_16x16x32_bf16 v[156:159], a[152:155], a[28:31], v[156:159]\n"
      "ds_read_b64_tr_b16 a[174:175], v52 offset:33792\n"
      "v_mfma_f32_16x16x32_bf16 v[188:191], a[156:159], a[28:31], v[188:191]\n"
      "buffer_load_dwordx4 v[240:243], v59, s[68:71], s77 offen offset:0\n"
      "v_mfma_f32_16x16x32_bf16 v[184:187], a[156:159], a[24:27], v[184:187]\n"
      "buffer_load_dwordx4 v[244:247], v59, s[68:71], s77 offen offset:64\n"
      "v_mfma_f32_16x16x32_bf16 v[180:183], a[156:159], a[20:23], v[180:183]\n"
      "buffer_load_dwordx4 v[248:251], v59, s[68:71], s78 offen offset:0\n"
      "v_mfma_f32_16x16x32_bf16 v[176:179], a[156:159], a[16:19], v[176:179]\n"
      "buffer_load_dwordx4 v[252:255], v59, s[68:71], s78 offen offset:64\n"
      "s_waitcnt lgkmcnt(0)\n"
      "v_mfma_f32_16x16x32_bf16 v[80:83], a[160:163], a[0:3], v[80:83]\n"
      "ds_read_b64_tr_b16 a[176:177], v49 offset:40960\n"
      "v_mfma_f32_16x16x32_bf16 v[84:87], a[160:163], a[4:7], v[84:87]\n"
      "ds_read_b64_tr_b16 a[178:179], v49 offset:41984\n"
      "v_mfma_f32_16x16x32_bf16 v[88:91], a[160:163], a[8:11], v[88:91]\n"
      "ds_read_b128 a[16:19], v21 offset:8192\n"
      "v_mfma_f32_16x16x32_bf16 v[92:95], a[160:163], a[12:15], v[92:95]\n"
      "ds_read_b128 a[20:23], v21 offset:10240\n"
      "v_mfma_f32_16x16x32_bf16 v[124:127], a[164:167], a[12:15], v[124:127]\n"
      "ds_read_b128 a[24:27], v21 offset:12288\n"
      "v_mfma_f32_16x16x32_bf16 v[120:123], a[164:167], a[8:11], v[120:123]\n"
      "ds_read_b128 a[28:31], v21 offset:14336\n"
      "v_mfma_f32_16x16x32_bf16 v[116:119], a[164:167], a[4:7], v[116:119]\n"
      "ds_read_b64_tr_b16 a[180:181], v50 offset:40960\n"
      "v_mfma_f32_16x16x32_bf16 v[112:115], a[164:167], a[0:3], v[112:115]\n"
      "ds_read_b64_tr_b16 a[182:183], v50 offset:41984\n"
      "v_mfma_f32_16x16x32_bf16 v[144:147], a[168:171], a[0:3], v[144:147]\n"
      "ds_read_b64_tr_b16 a[184:185], v51 offset:40960\n"
      "v_mfma_f32_16x16x32_bf16 v[148:151], a[168:171], a[4:7], v[148:151]\n"
      "ds_read_b64_tr_b16 a[186:187], v51 offset:41984\n"
      "v_mfma_f32_16x16x32_bf16 v[152:155], a[168:171], a[8:11], v[152:155]\n"
      "ds_read_b64_tr_b16 a[188:189], v52 offset:40960\n"
      "v_mfma_f32_16x16x32_bf16 v[156:159], a[168:171], a[12:15], v[156:159]\n"
      "ds_read_b64_tr_b16 a[190:191], v52 offset:41984\n"
      "v_mfma_f32_16x16x32_bf16 v[188:191], a[172:175], a[12:15], v[188:191]\n"
      "v_mfma_f32_16x16x32_bf16 v[184:187], a[172:175], a[8:11], v[184:187]\n"
      "v_mfma_f32_16x16x32_bf16 v[180:183], a[172:175], a[4:7], v[180:183]\n"
      "v_mfma_f32_16x16x32_bf16 v[176:179], a[172:175], a[0:3], v[176:179]\n"
      "s_waitcnt lgkmcnt(0)\n"
      "v_mfma_f32_16x16x32_bf16 v[92:95], a[176:179], a[28:31], v[92:95]\n"
      "ds_read_b128 a[32:35], v16 offset:4096\n"
      "v_mfma_f32_16x16x32_bf16 v[124:127], a[180:183], a[28:31], v[124:127]\n"
      "ds_read_b128 a[36:39], v16 offset:6144\n"
      "v_mfma_f32_16x16x32_bf16 v[156:159], a[184:187], a[28:31], v[156:159]\n"
      "ds_read_b128 a[40:43], v17 offset:4096\n"
      "v_mfma_f32_16x16x32_bf16 v[188:191], a[188:191], a[28:31], v[188:191]\n"
      "ds_read_b128 a[44:47], v17 offset:6144\n"
      "v_mfma_f32_16x16x32_bf16 v[184:187], a[188:191], a[24:27], v[184:187]\n"
      "ds_read_b128 a[48:51], v18 offset:4096\n"
      "v_mfma_f32_16x16x32_bf16 v[152:155], a[184:187], a[24:27], v[152:155]\n"
      "ds_read_b128 a[52:55], v18 offset:6144\n"
      "v_mfma_f32_16x16x32_bf16 v[120:123], a[180:183], a[24:27], v[120:123]\n"
      "ds_read_b128 a[56:59], v19 offset:4096\n"
      "v_mfma_f32_16x16x32_bf16 v[88:91], a[176:179], a[24:27], v[88:91]\n"
      "ds_read_b128 a[60:63], v19 offset:6144\n"
      "v_mfma_f32_16x16x32_bf16 v[84:87], a[176:179], a[20:23], v[84:87]\n"
      "ds_read_b128 a[64:67], v20 offset:4096\n"
      "v_mfma_f32_16x16x32_bf16 v[116:119], a[180:183], a[20:23], v[116:119]\n"
      "ds_read_b128 a[68:71], v20 offset:6144\n"
      "v_mfma_f32_16x16x32_bf16 v[148:151], a[184:187], a[20:23], v[148:151]\n"
      "ds_read_b128 a[72:75], v21 offset:4096\n"
      "v_mfma_f32_16x16x32_bf16 v[180:183], a[188:191], a[20:23], v[180:183]\n"
      "ds_read_b128 a[76:79], v21 offset:6144\n"
      "v_mfma_f32_16x16x32_bf16 v[176:179], a[188:191], a[16:19], v[176:179]\n"
      "v_mfma_f32_16x16x32_bf16 v[144:147], a[184:187], a[16:19], v[144:147]\n"
      "v_mfma_f32_16x16x32_bf16 v[112:115], a[180:183], a[16:19], v[112:115]\n"
      "v_mfma_f32_16x16x32_bf16 v[80:83], a[176:179], a[16:19], v[80:83]\n"
      "s_waitcnt lgkmcnt(0)\n"
      "v_mfma_f32_16x16x32_bf16 v[76:79], a[96:99], a[36:39], v[76:79]\n"
      "v_cvt_pk_bf16_f32 v92, v92, v93\n"
      "v_cvt_pk_bf16_f32 v93, v94, v95\n"
      "v_mfma_f32_16x16x32_bf16 v[108:111], a[100:103], a[36:39], v[108:111]\n"
      "v_cvt_pk_bf16_f32 v94, v124, v125\n"
      "v_cvt_pk_bf16_f32 v95, v126, v127\n"
      "v_mfma_f32_16x16x32_bf16 v[140:143], a[104:107], a[36:39], v[140:143]\n"
      "ds_read_b128 a[0:3], v16 offset:0\n"
      "buffer_store_dwordx4 v[92:95], v58, s[64:67], s75 offen offset:2048 sc1\n"
      "v_mfma_f32_16x16x32_bf16 v[172:175], a[108:111], a[36:39], v[172:175]\n"
      "v_cvt_pk_bf16_f32 v156, v156, v157\n"
      "v_cvt_pk_bf16_f32 v157, v158, v159\n"
      "v_mfma_f32_16x16x32_bf16 v[168:171], a[108:111], a[32:35], v[168:171]\n"
      "v_cvt_pk_bf16_f32 v158, v188, v189\n"
      "v_cvt_pk_bf16_f32 v159, v190, v191\n"
      "v_mfma_f32_16x16x32_bf16 v[136:139], a[104:107], a[32:35], v[136:139]\n"
      "ds_read_b128 a[4:7], v16 offset:2048\n"
      "v_mfma_f32_16x16x32_bf16 v[104:107], a[100:103], a[32:35], v[104:107]\n"
      "v_mfma_f32_16x16x32_bf16 v[72:75], a[96:99], a[32:35], v[72:75]\n"
      "v_mfma_f32_16x16x32_bf16 v[76:79], a[112:115], a[44:47], v[76:79]\n"
      "ds_read_b128 a[8:11], v17 offset:0\n"
      "buffer_store_dwordx4 v[156:159], v58, s[64:67], s75 offen offset:3072 sc1\n"
      "v_mfma_f32_16x16x32_bf16 v[108:111], a[116:119], a[44:47], v[108:111]\n"
      "v_cvt_pk_bf16_f32 v88, v88, v89\n"
      "v_cvt_pk_bf16_f32 v89, v90, v91\n"
      "v_mfma_f32_16x16x32_bf16 v[140:143], a[120:123], a[44:47], v[140:143]\n"
      "v_cvt_pk_bf16_f32 v90, v120, v121\n"
      "v_cvt_pk_bf16_f32 v91, v122, v123\n"
      "v_mfma_f32_16x16x32_bf16 v[172:175], a[124:127], a[44:47], v[172:175]\n"
      "ds_read_b128 a[12:15], v17 offset:2048\n"
      "v_mfma_f32_16x16x32_bf16 v[168:171], a[124:127], a[40:43], v[168:171]\n"
      "v_mfma_f32_16x16x32_bf16 v[136:139], a[120:123], a[40:43], v[136:139]\n"
      "v_mfma_f32_16x16x32_bf16 v[104:107], a[116:119], a[40:43], v[104:107]\n"
      "ds_read_b128 a[16:19], v18 offset:0\n"
      "buffer_store_dwordx4 v[88:91], v58, s[64:67], s75 offen offset:0 sc1\n"
      "v_mfma_f32_16x16x32_bf16 v[72:75], a[112:115], a[40:43], v[72:75]\n"
      "v_cvt_pk_bf16_f32 v152, v152, v153\n"
      "v_cvt_pk_bf16_f32 v153, v154, v155\n"
      "v_mfma_f32_16x16x32_bf16 v[76:79], a[128:131], a[52:55], v[76:79]\n"
      "v_cvt_pk_bf16_f32 v154, v184, v185\n"
      "v_cvt_pk_bf16_f32 v155, v186, v187\n"
      "v_mfma_f32_16x16x32_bf16 v[108:111], a[132:135], a[52:55], v[108:111]\n"
      "ds_read_b128 a[20:23], v18 offset:2048\n"
      "v_mfma_f32_16x16x32_bf16 v[140:143], a[136:139], a[52:55], v[140:143]\n"
      "v_mfma_f32_16x16x32_bf16 v[172:175], a[140:143], a[52:55], v[172:175]\n"
      "v_mfma_f32_16x16x32_bf16 v[168:171], a[140:143], a[48:51], v[168:171]\n"
      "ds_read_b128 a[24:27], v19 offset:0\n"
      "buffer_store_dwordx4 v[152:155], v58, s[64:67], s75 offen offset:1024 sc1\n"
      "v_mfma_f32_16x16x32_bf16 v[136:139], a[136:139], a[48:51], v[136:139]\n"
      "v_cvt_pk_bf16_f32 v84, v84, v85\n"
      "v_cvt_pk_bf16_f32 v85, v86, v87\n"
      "v_mfma_f32_16x16x32_bf16 v[104:107], a[132:135], a[48:51], v[104:107]\n"
      "v_cvt_pk_bf16_f32 v86, v116, v117\n"
      "v_cvt_pk_bf16_f32 v87, v118, v119\n"
      "v_mfma_f32_16x16x32_bf16 v[72:75], a[128:131], a[48:51], v[72:75]\n"
      "ds_read_b128 a[28:31], v19 offset:2048\n"
      "v_mfma_f32_16x16x32_bf16 v[76:79], a[144:147], a[60:63], v[76:79]\n"
      "v_mfma_f32_16x16x32_bf16 v[108:111], a[148:151], a[60:63], v[108:111]\n"
      "v_mfma_f32_16x16x32_bf16 v[140:143], a[152:155], a[60:63], v[140:143]\n"
      "ds_read_b128 a[80:83], v20 offset:0\n"
      "buffer_store_dwordx4 v[84:87], v58, s[64:67], s74 offen offset:2048 sc1\n"
      "v_mfma_f32_16x16x32_bf16 v[172:175], a[156:159], a[60:63], v[172:175]\n"
      "v_cvt_pk_bf16_f32 v148, v148, v149\n"
      "v_cvt_pk_bf16_f32 v149, v150, v151\n"
      "v_mfma_f32_16x16x32_bf16 v[168:171], a[156:159], a[56:59], v[168:171]\n"
      "v_cvt_pk_bf16_f32 v150, v180, v181\n"
      "v_cvt_pk_bf16_f32 v151, v182, v183\n"
      "v_mfma_f32_16x16x32_bf16 v[136:139], a[152:155], a[56:59], v[136:139]\n"
      "ds_read_b128 a[84:87], v20 offset:2048\n"
      "v_mfma_f32_16x16x32_bf16 v[104:107], a[148:151], a[56:59], v[104:107]\n"
      "v_mfma_f32_16x16x32_bf16 v[72:75], a[144:147], a[56:59], v[72:75]\n"
      "v_mfma_f32_16x16x32_bf16 v[76:79], a[160:163], a[68:71], v[76:79]\n"
      "ds_read_b128 a[88:91], v21 offset:0\n"
      "buffer_store_dwordx4 v[148:151], v58, s[64:67], s74 offen offset:3072 sc1\n"
      "v_mfma_f32_16x16x32_bf16 v[108:111], a[164:167], a[68:71], v[108:111]\n"
      "v_cvt_pk_bf16_f32 v80, v80, v81\n"
      "v_cvt_pk_bf16_f32 v81, v82, v83\n"
      "v_mfma_f32_16x16x32_bf16 v[140:143], a[168:171], a[68:71], v[140:143]\n"
      "v_cvt_pk_bf16_f32 v82, v112, v113\n"
      "v_cvt_pk_bf16_f32 v83, v114, v115\n"
      "v_mfma_f32_16x16x32_bf16 v[172:175], a[172:175], a[68:71], v[172:175]\n"
      "ds_read_b128 a[92:95], v21 offset:2048\n"
      "v_mfma_f32_16x16x32_bf16 v[168:171], a[172:175], a[64:67], v[168:171]\n"
      "v_mfma_f32_16x16x32_bf16 v[136:139], a[168:171], a[64:67], v[136:139]\n"
      "v_mfma_f32_16x16x32_bf16 v[104:107], a[164:167], a[64:67], v[104:107]\n"
      "buffer_store_dwordx4 v[80:83], v58, s[64:67], s74 offen offset:0 sc1\n"
      "v_cvt_pk_bf16_f32 v144, v144, v145\n"
      "v_mfma_f32_16x16x32_bf16 v[72:75], a[160:163], a[64:67], v[72:75]\n"
      "v_cvt_pk_bf16_f32 v145, v146, v147\n"
      "v_cvt_pk_bf16_f32 v146, v176, v177\n"
      "v_mfma_f32_16x16x32_bf16 v[76:79], a[176:179], a[76:79], v[76:79]\n"
      "v_cvt_pk_bf16_f32 v147, v178, v179\n"
      "v_mfma_f32_16x16x32_bf16 v[108:111], a[180:183], a[76:79], v[108:111]\n"
      "v_mfma_f32_16x16x32_bf16 v[140:143], a[184:187], a[76:79], v[140:143]\n"
      "v_mfma_f32_16x16x32_bf16 v[172:175], a[188:191], a[76:79], v[172:175]\n"
      "v_mfma_f32_16x16x32_bf16 v[168:171], a[188:191], a[72:75], v[168:171]\n"
      "buffer_store_dwordx4 v[144:147], v58, s[64:67], s74 offen offset:1024 sc1\n"
      "v_mfma_f32_16x16x32_bf16 v[136:139], a[184:187], a[72:75], v[136:139]\n"
      "v_mfma_f32_16x16x32_bf16 v[104:107], a[180:183], a[72:75], v[104:107]\n"
      "v_mfma_f32_16x16x32_bf16 v[72:75], a[176:179], a[72:75], v[72:75]\n"
      "s_waitcnt lgkmcnt(0)\n"
      "v_mfma_f32_16x16x32_bf16 v[64:67], a[96:99], a[0:3], v[64:67]\n"
      "v_mfma_f32_16x16x32_bf16 v[96:99], a[100:103], a[0:3], v[96:99]\n"
      "v_mfma_f32_16x16x32_bf16 v[128:131], a[104:107], a[0:3], v[128:131]\n"
      "v_mfma_f32_16x16x32_bf16 v[160:163], a[108:111], a[0:3], v[160:163]\n"
      "v_mfma_f32_16x16x32_bf16 v[164:167], a[108:111], a[4:7], v[164:167]\n"
      "v_cvt_pk_bf16_f32 v76, v76, v77\n"
      "v_cvt_pk_bf16_f32 v77, v78, v79\n"
      "v_mfma_f32_16x16x32_bf16 v[132:135], a[104:107], a[4:7], v[132:135]\n"
      "v_cvt_pk_bf16_f32 v78, v108, v109\n"
      "v_cvt_pk_bf16_f32 v79, v110, v111\n"
      "v_mfma_f32_16x16x32_bf16 v[100:103], a[100:103], a[4:7], v[100:103]\n"
      "buffer_store_dwordx4 v[76:79], v58, s[64:67], s73 offen offset:2048 sc1\n"
      "v_cvt_pk_bf16_f32 v140, v140, v141\n"
      "v_mfma_f32_16x16x32_bf16 v[68:71], a[96:99], a[4:7], v[68:71]\n"
      "v_cvt_pk_bf16_f32 v141, v142, v143\n"
      "v_cvt_pk_bf16_f32 v142, v172, v173\n"
      "v_mfma_f32_16x16x32_bf16 v[64:67], a[112:115], a[8:11], v[64:67]\n"
      "v_cvt_pk_bf16_f32 v143, v174, v175\n"
      "v_mfma_f32_16x16x32_bf16 v[96:99], a[116:119], a[8:11], v[96:99]\n"
      "v_mfma_f32_16x16x32_bf16 v[128:131], a[120:123], a[8:11], v[128:131]\n"
      "v_mfma_f32_16x16x32_bf16 v[160:163], a[124:127], a[8:11], v[160:163]\n"
      "v_mfma_f32_16x16x32_bf16 v[164:167], a[124:127], a[12:15], v[164:167]\n"
      "buffer_store_dwordx4 v[140:143], v58, s[64:67], s73 offen offset:3072 sc1\n"
      "v_cvt_pk_bf16_f32 v72, v72, v73\n"
      "v_mfma_f32_16x16x32_bf16 v[132:135], a[120:123], a[12:15], v[132:135]\n"
      "v_cvt_pk_bf16_f32 v73, v74, v75\n"
      "v_cvt_pk_bf16_f32 v74, v104, v105\n"
      "v_mfma_f32_16x16x32_bf16 v[100:103], a[116:119], a[12:15], v[100:103]\n"
      "v_cvt_pk_bf16_f32 v75, v106, v107\n"
      "v_mfma_f32_16x16x32_bf16 v[68:71], a[112:115], a[12:15], v[68:71]\n"
      "v_mfma_f32_16x16x32_bf16 v[64:67], a[128:131], a[16:19], v[64:67]\n"
      "v_mfma_f32_16x16x32_bf16 v[96:99], a[132:135], a[16:19], v[96:99]\n"
      "v_mfma_f32_16x16x32_bf16 v[128:131], a[136:139], a[16:19], v[128:131]\n"
      "buffer_store_dwordx4 v[72:75], v58, s[64:67], s73 offen offset:0 sc1\n"
      "v_cvt_pk_bf16_f32 v136, v136, v137\n"
      "v_mfma_f32_16x16x32_bf16 v[160:163], a[140:143], a[16:19], v[160:163]\n"
      "v_cvt_pk_bf16_f32 v137, v138, v139\n"
      "v_cvt_pk_bf16_f32 v138, v168, v169\n"
      "v_mfma_f32_16x16x32_bf16 v[164:167], a[140:143], a[20:23], v[164:167]\n"
      "v_cvt_pk_bf16_f32 v139, v170, v171\n"
      "v_mfma_f32_16x16x32_bf16 v[132:135], a[136:139], a[20:23], v[132:135]\n"
      "v_mfma_f32_16x16x32_bf16 v[100:103], a[132:135], a[20:23], v[100:103]\n"
      "v_mfma_f32_16x16x32_bf16 v[68:71], a[128:131], a[20:23], v[68:71]\n"
      "v_mfma_f32_16x16x32_bf16 v[64:67], a[144:147], a[24:27], v[64:67]\n"
      "buffer_store_dwordx4 v[136:139], v58, s[64:67], s73 offen offset:1024 sc1\n"
      "v_mfma_f32_16x16x32_bf16 v[96:99], a[148:151], a[24:27], v[96:99]\n"
      "v_mfma_f32_16x16x32_bf16 v[128:131], a[152:155], a[24:27], v[128:131]\n"
      "v_mfma_f32_16x16x32_bf16 v[160:163], a[156:159], a[24:27], v[160:163]\n"
      "v_mfma_f32_16x16x32_bf16 v[164:167], a[156:159], a[28:31], v[164:167]\n"
      "v_mfma_f32_16x16x32_bf16 v[132:135], a[152:155], a[28:31], v[132:135]\n"
      "v_mfma_f32_16x16x32_bf16 v[100:103], a[148:151], a[28:31], v[100:103]\n"
      "v_mfma_f32_16x16x32_bf16 v[68:71], a[144:147], a[28:31], v[68:71]\n"
      "v_mfma_f32_16x16x32_bf16 v[64:67], a[160:163], a[80:83], v[64:67]\n"
      "v_mfma_f32_16x16x32_bf16 v[96:99], a[164:167], a[80:83], v[96:99]\n"
      "v_mfma_f32_16x16x32_bf16 v[128:131], a[168:171], a[80:83], v[128:131]\n"
      "v_mfma_f32_16x16x32_bf16 v[160:163], a[172:175], a[80:83], v[160:163]\n"
      "v_mfma_f32_16x16x32_bf16 v[164:167], a[172:175], a[84:87], v[164:167]\n"
      "v_mfma_f32_16x16x32_bf16 v[132:135], a[168:171], a[84:87], v[132:135]\n"
      "v_mfma_f32_16x16x32_bf16 v[100:103], a[164:167], a[84:87], v[100:103]\n"
      "v_mfma_f32_16x16x32_bf16 v[68:71], a[160:163], a[84:87], v[68:71]\n"
      "v_mfma_f32_16x16x32_bf16 v[64:67], a[176:179], a[88:91], v[64:67]\n"
      "s_waitcnt vmcnt(4)\n"
      "buffer_store_dwordx2 v[6:7], v5, s[64:67], s79 offen sc1\n"
      "v_mfma_f32_16x16x32_bf16 v[96:99], a[180:183], a[88:91], v[96:99]\n"
      "v_permlane16_swap_b32 v240, v242\n"
      "v_permlane16_swap_b32 v241, v243\n"
      "v_mfma_f32_16x16x32_bf16 v[128:131], a[184:187], a[88:91], v[128:131]\n"
      "v_permlane16_swap_b32 v244, v246\n"
      "v_permlane16_swap_b32 v245, v247\n"
      "v_mfma_f32_16x16x32_bf16 v[160:163], a[188:191], a[88:91], v[160:163]\n"
      "v_permlane16_swap_b32 v248, v250\n"
      "v_permlane16_swap_b32 v249, v251\n"
      "v_mfma_f32_16x16x32_bf16 v[164:167], a[188:191], a[92:95], v[164:167]\n"
      "v_permlane16_swap_b32 v252, v254\n"
      "v_permlane16_swap_b32 v253, v255\n"
      "v_mfma_f32_16x16x32_bf16 v[132:135], a[184:187], a[92:95], v[132:135]\n"
      "v_lshlrev_b32 v80, 16, v240\n"
      "v_and_b32 v81, 0xffff0000, v240\n"
      "v_mfma_f32_16x16x32_bf16 v[100:103], a[180:183], a[92:95], v[100:103]\n"
      "v_lshlrev_b32 v82, 16, v241\n"
      "v_and_b32 v83, 0xffff0000, v241\n"
      "v_mfma_f32_16x16x32_bf16 v[68:71], a[176:179], a[92:95], v[68:71]\n"
      "v_lshlrev_b32 v112, 16, v242\n"
      "v_and_b32 v113, 0xffff0000, v242\n"
      "v_lshlrev_b32 v114, 16, v243\n"
      "v_and_b32 v115, 0xffff0000, v243\n"
      "v_lshlrev_b32 v144, 16, v244\n"
      "v_and_b32 v145, 0xffff0000, v244\n"
      "v_lshlrev_b32 v146, 16, v245\n"
      "v_and_b32 v147, 0xffff0000, v245\n"
      "v_lshlrev_b32 v176, 16, v246\n"
      "v_and_b32 v177, 0xffff0000, v246\n"
      "v_lshlrev_b32 v178, 16, v247\n"
      "v_and_b32 v179, 0xffff0000, v247\n"
      "v_lshlrev_b32 v84, 16, v248\n"
      "v_and_b32 v85, 0xffff0000, v248\n"
      "v_lshlrev_b32 v86, 16, v249\n"
      "v_and_b32 v87, 0xffff0000, v249\n"
      "v_lshlrev_b32 v116, 16, v250\n"
      "v_and_b32 v117, 0xffff0000, v250\n"
      "v_lshlrev_b32 v118, 16, v251\n"
      "v_and_b32 v119, 0xffff0000, v251\n"
      "v_lshlrev_b32 v148, 16, v252\n"
      "v_and_b32 v149, 0xffff0000, v252\n"
      "v_lshlrev_b32 v150, 16, v253\n"
      "v_and_b32 v151, 0xffff0000, v253\n"
      "v_lshlrev_b32 v180, 16, v254\n"
      "v_and_b32 v181, 0xffff0000, v254\n"
      "v_lshlrev_b32 v182, 16, v255\n"
      "v_and_b32 v183, 0xffff0000, v255\n"
      "s_add_u32 s79, s79, 0x20000\n"
      "s_waitcnt vmcnt(1)\n"
      "buffer_store_dwordx2 v[6:7], v5, s[64:67], s79 offen sc1\n"
      "buffer_load_dwordx2 v[56:57], v55, s[64:67], s80 offen sc1\n"
      "s_mov_b32 s81, 0\n"
      "s_mov_b32 s82, 0\n"
      "s_mov_b32 s84, 0x40000\n"
      "s_nop 4\n"
      "v_pk_add_f32 v[64:65], v[64:65], v[80:81]\n"
      "v_pk_add_f32 v[66:67], v[66:67], v[82:83]\n"
      "v_pk_add_f32 v[96:97], v[96:97], v[112:113]\n"
      "v_pk_add_f32 v[98:99], v[98:99], v[114:115]\n"
      "v_pk_add_f32 v[128:129], v[128:129], v[144:145]\n"
      "v_pk_add_f32 v[130:131], v[130:131], v[146:147]\n"
      "v_pk_add_f32 v[160:161], v[160:161], v[176:177]\n"
      "v_pk_add_f32 v[162:163], v[162:163], v[178:179]\n"
      "v_pk_add_f32 v[68:69], v[68:69], v[84:85]\n"
      "v_pk_add_f32 v[70:71], v[70:71], v[86:87]\n"
      "v_pk_add_f32 v[100:101], v[100:101], v[116:117]\n"
      "v_pk_add_f32 v[102:103], v[102:103], v[118:119]\n"
      "v_pk_add_f32 v[132:133], v[132:133], v[148:149]\n"
      "v_pk_add_f32 v[134:135], v[134:135], v[150:151]\n"
      "v_pk_add_f32 v[164:165], v[164:165], v[180:181]\n"
      "v_pk_add_f32 v[166:167], v[166:167], v[182:183]\n"
      "Lk_xl:\n"
      "s_waitcnt vmcnt(0)\n"
      "v_cmp_eq_u64 vcc, s[54:55], v[56:57]\n"
      "s_sub_u32 s84, s84, 1\n"
      "s_cselect_b32 s83, 7, vcc_lo\n"
      "s_and_b32 s83, s83, 7\n"
      "s_andn2_b32 s83, s83, s81\n"
      "s_bitcmp1_b32 s83, 1\n"
      "s_cbranch_scc0 Lk_xq_24\n"
      "s_add_u32 s72, s76, 32768\n"
      "buffer_load_dwordx4 v[208:211], v58, s[64:67], s72 offen offset:0 sc1\n"
      "buffer_load_dwordx4 v[212:215], v58, s[64:67], s72 offen offset:1024 sc1\n"
      "buffer_load_dwordx4 v[216:219], v58, s[64:67], s72 offen offset:2048 sc1\n"
      "buffer_load_dwordx4 v[220:223], v58, s[64:67], s72 offen offset:3072 sc1\n"
      "Lk_xq_24:\n"
      "s_bitcmp1_b32 s83, 2\n"
      "s_cbranch_scc0 Lk_xq_25\n"
      "s_add_u32 s72, s76, 49152\n"
      "buffer_load_dwordx4 v[224:227], v58, s[64:67], s72 offen offset:0 sc1\n"
      "buffer_load_dwordx4 v[228:231], v58, s[64:67], s72 offen offset:1024 sc1\n"
      "buffer_load_dwordx4 v[232:235], v58, s[64:67], s72 offen offset:2048 sc1\n"
      "buffer_load_dwordx4 v[236:239], v58, s[64:67], s72 offen offset:3072 sc1\n"
      "Lk_xq_25:\n"
      "s_bitcmp1_b32 s83, 0\n"
      "s_cbranch_scc0 Lk_xq_26\n"
      "s_add_u32 s72, s76, 16384\n"
      "buffer_load_dwordx4 v[192:195], v58, s[64:67], s72 offen offset:0 sc1\n"
      "buffer_load_dwordx4 v[196:199], v58, s[64:67], s72 offen offset:1024 sc1\n"
      "buffer_load_dwordx4 v[200:203], v58, s[64:67], s72 offen offset:2048 sc1\n"
      "buffer_load_dwordx4 v[204:207], v58, s[64:67], s72 offen offset:3072 sc1\n"
      "Lk_xq_26:\n"
      "s_or_b32 s81, s81, s83\n"
      "buffer_load_dwordx2 v[56:57], v55, s[64:67], s80 offen sc1\n"
      "s_waitcnt vmcnt(1)\n"
      "s_bitcmp1_b32 s83, 1\n"
      "s_cbranch_scc0 Lk_xa_27\n"
      "v_lshlrev_b32 v80, 16, v208\n"
      "v_and_b32 v81, 0xffff0000, v208\n"
      "v_lshlrev_b32 v82, 16, v209\n"
      "v_and_b32 v83, 0xffff0000, v209\n"
      "v_lshlrev_b32 v112, 16, v210\n"
      "v_and_b32 v113, 0xffff0000, v210\n"
      "v_lshlrev_b32 v114, 16, v211\n"
      "v_and_b32 v115, 0xffff0000, v211\n"
      "v_lshlrev_b32 v144, 16, v212\n"
      "v_and_b32 v145, 0xffff0000, v212\n"
      "v_lshlrev_b32 v146, 16, v213\n"
      "v_and_b32 v147, 0xffff0000, v213\n"
      "v_lshlrev_b32 v176, 16, v214\n"
      "v_and_b32 v177, 0xffff0000, v214\n"
      "v_lshlrev_b32 v178, 16, v215\n"
      "v_and_b32 v179, 0xffff0000, v215\n"
      "v_lshlrev_b32 v84, 16, v216\n"
      "v_and_b32 v85, 0xffff0000, v216\n"
      "v_lshlrev_b32 v86, 16, v217\n"
      "v_and_b32 v87, 0xffff0000, v217\n"
      "v_lshlrev_b32 v116, 16, v218\n"
      "v_and_b32 v117, 0xffff0000, v218\n"
      "v_lshlrev_b32 v118, 16, v219\n"
      "v_and_b32 v119, 0xffff0000, v219\n"
      "v_lshlrev_b32 v148, 16, v220\n"
      "v_and_b32 v149, 0xffff0000, v220\n"
      "v_lshlrev_b32 v150, 16, v221\n"
      "v_and_b32 v151, 0xffff0000, v221\n"
      "v_lshlrev_b32 v180, 16, v222\n"
      "v_and_b32 v181, 0xffff0000, v222\n"
      "v_lshlrev_b32 v182, 16, v223\n"
      "v_and_b32 v183, 0xffff0000, v223\n"
      "Lk_xa_27:\n"
      "s_bitcmp1_b32 s83, 2\n"
      "s_cbranch_scc0 Lk_xa_28\n"
      "v_lshlrev_b32 v88, 16, v224\n"
      "v_and_b32 v89, 0xffff0000, v224\n"
      "v_lshlrev_b32 v90, 16, v225\n"
      "v_and_b32 v91, 0xffff0000, v225\n"
      "v_lshlrev_b32 v120, 16, v226\n"
      "v_and_b32 v121, 0xffff0000, v226\n"
      "v_lshlrev_b32 v122, 16, v227\n"
      "v_and_b32 v123, 0xffff0000, v227\n"
      "v_lshlrev_b32 v152, 16, v228\n"
      "v_and_b32 v153, 0xffff0000, v228\n"
      "v_lshlrev_b32 v154, 16, v229\n"
      "v_and_b32 v155, 0xffff0000, v229\n"
      "v_lshlrev_b32 v184, 16, v230\n"
      "v_and_b32 v185, 0xffff0000, v230\n"
      "v_lshlrev_b32 v186, 16, v231\n"
      "v_and_b32 v187, 0xffff0000, v231\n"
      "v_lshlrev_b32 v92, 16, v232\n"
      "v_and_b32 v93, 0xffff0000, v232\n"
      "v_lshlrev_b32 v94, 16, v233\n"
      "v_and_b32 v95, 0xffff0000, v233\n"
      "v_lshlrev_b32 v124, 16, v234\n"
      "v_and_b32 v125, 0xffff0000, v234\n"
      "v_lshlrev_b32 v126, 16, v235\n"
      "v_and_b32 v127, 0xffff0000, v235\n"
      "v_lshlrev_b32 v156, 16, v236\n"
      "v_and_b32 v157, 0xffff0000, v236\n"
      "v_lshlrev_b32 v158, 16, v237\n"
      "v_and_b32 v159, 0xffff0000, v237\n"
      "v_lshlrev_b32 v188, 16, v238\n"
      "v_and_b32 v189, 0xffff0000, v238\n"
      "v_lshlrev_b32 v190, 16, v239\n"
      "v_and_b32 v191, 0xffff0000, v239\n"
      "Lk_xa_28:\n"
      "s_bitcmp1_b32 s83, 0\n"
      "s_cbranch_scc0 Lk_xa_29\n"
      "v_lshlrev_b32 v72, 16, v192\n"
      "v_and_b32 v73, 0xffff0000, v192\n"
      "v_lshlrev_b32 v74, 16, v193\n"
      "v_and_b32 v75, 0xffff0000, v193\n"
      "v_lshlrev_b32 v104, 16, v194\n"
      "v_and_b32 v105, 0xffff0000, v194\n"
      "v_lshlrev_b32 v106, 16, v195\n"
      "v_and_b32 v107, 0xffff0000, v195\n"
      "v_lshlrev_b32 v136, 16, v196\n"
      "v_and_b32 v137, 0xffff0000, v196\n"
      "v_lshlrev_b32 v138, 16, v197\n"
      "v_and_b32 v139, 0xffff0000, v197\n"
      "v_lshlrev_b32 v168, 16, v198\n"
      "v_and_b32 v169, 0xffff0000, v198\n"
      "v_lshlrev_b32 v170, 16, v199\n"
      "v_and_b32 v171, 0xffff0000, v199\n"
      "v_lshlrev_b32 v76, 16, v200\n"
      "v_and_b32 v77, 0xffff0000, v200\n"
      "v_lshlrev_b32 v78, 16, v201\n"
      "v_and_b32 v79, 0xffff0000, v201\n"
      "v_lshlrev_b32 v108, 16, v202\n"
      "v_and_b32 v109, 0xffff0000, v202\n"
      "v_lshlrev_b32 v110, 16, v203\n"
      "v_and_b32 v111, 0xffff0000, v203\n"
      "v_lshlrev_b32 v140, 16, v204\n"
      "v_and_b32 v141, 0xffff0000, v204\n"
      "v_lshlrev_b32 v142, 16, v205\n"
      "v_and_b32 v143, 0xffff0000, v205\n"
      "v_lshlrev_b32 v172, 16, v206\n"
      "v_and_b32 v173, 0xffff0000, v206\n"
      "v_lshlrev_b32 v174, 16, v207\n"
      "v_and_b32 v175, 0xffff0000, v207\n"
      "Lk_xa_29:\n"
      "s_cmp_eq_u32 s81, 7\n"
      "s_cbranch_scc0 Lk_xl\n"
      "v_pk_add_f32 v[64:65], v[64:65], v[80:81]\n"
      "v_pk_add_f32 v[66:67], v[66:67], v[82:83]\n"
      "v_pk_add_f32 v[96:97], v[96:97], v[112:113]\n"
      "v_pk_add_f32 v[98:99], v[98:99], v[114:115]\n"
      "v_pk_add_f32 v[64:65], v[64:65], v[88:89]\n"
      "v_pk_add_f32 v[66:67], v[66:67], v[90:91]\n"
      "v_pk_add_f32 v[96:97], v[96:97], v[120:121]\n"
      "v_pk_add_f32 v[98:99], v[98:99], v[122:123]\n"
      "v_pk_add_f32 v[64:65], v[64:65], v[72:73]\n"
      "v_pk_add_f32 v[66:67], v[66:67], v[74:75]\n"
      "v_pk_add_f32 v[96:97], v[96:97], v[104:105]\n"
      "v_pk_add_f32 v[98:99], v[98:99], v[106:107]\n"
      "v_cvt_pk_bf16_f32 v64, v64, v65\n"
      "v_cvt_pk_bf16_f32 v65, v66, v67\n"
      "v_cvt_pk_bf16_f32 v66, v96, v97\n"
      "v_cvt_pk_bf16_f32 v67, v98, v99\n"
      "s_nop 0\n"
      "v_permlane16_swap_b32 v64, v66\n"
      "v_permlane16_swap_b32 v65, v67\n"
      "buffer_store_dwordx4 v[64:67], v59, s[24:27], s77 offen offset:0 sc1\n"
      "v_pk_add_f32 v[128:129], v[128:129], v[144:145]\n"
      "v_pk_add_f32 v[130:131], v[130:131], v[146:147]\n"
      "v_pk_add_f32 v[160:161], v[160:161], v[176:177]\n"
      "v_pk_add_f32 v[162:163], v[162:163], v[178:179]\n"
      "v_pk_add_f32 v[128:129], v[128:129], v[152:153]\n"
      "v_pk_add_f32 v[130:131], v[130:131], v[154:155]\n"
      "v_pk_add_f32 v[160:161], v[160:161], v[184:185]\n"
      "v_pk_add_f32 v[162:163], v[162:163], v[186:187]\n"
      "v_pk_add_f32 v[128:129], v[128:129], v[136:137]\n"
      "v_pk_add_f32 v[130:131], v[130:131], v[138:139]\n"
      "v_pk_add_f32 v[160:161], v[160:161], v[168:169]\n"
      "v_pk_add_f32 v[162:163], v[162:163], v[170:171]\n"
      "v_cvt_pk_bf16_f32 v128, v128, v129\n"
      "v_cvt_pk_bf16_f32 v129, v130, v131\n"
      "v_cvt_pk_bf16_f32 v130, v160, v161\n"
      "v_cvt_pk_bf16_f32 v131, v162, v163\n"
      "s_nop 0\n"
      "v_permlane16_swap_b32 v128, v130\n"
      "v_permlane16_swap_b32 v129, v131\n"
      "buffer_store_dwordx4 v[128:131], v59, s[24:27], s77 offen offset:64 sc1\n"
      "v_pk_add_f32 v[68:69], v[68:69], v[84:85]\n"
      "v_pk_add_f32 v[70:71], v[70:71], v[86:87]\n"
      "v_pk_add_f32 v[100:101], v[100:101], v[116:117]\n"
      "v_pk_add_f32 v[102:103], v[102:103], v[118:119]\n"
      "v_pk_add_f32 v[68:69], v[68:69], v[92:93]\n"
      "v_pk_add_f32 v[70:71], v[70:71], v[94:95]\n"
      "v_pk_add_f32 v[100:101], v[100:101], v[124:125]\n"
      "v_pk_add_f32 v[102:103], v[102:103], v[126:127]\n"
      "v_pk_add_f32 v[68:69], v[68:69], v[76:77]\n"
      "v_pk_add_f32 v[70:71], v[70:71], v[78:79]\n"
      "v_pk_add_f32 v[100:101], v[100:101], v[108:109]\n"
      "v_pk_add_f32 v[102:103], v[102:103], v[110:111]\n"
      "v_cvt_pk_bf16_f32 v68, v68, v69\n"
      "v_cvt_pk_bf16_f32 v69, v70, v71\n"
      "v_cvt_pk_bf16_f32 v70, v100, v101\n"
      "v_cvt_pk_bf16_f32 v71, v102, v103\n"
      "s_nop 0\n"
      "v_permlane16_swap_b32 v68, v70\n"
      "v_permlane16_swap_b32 v69, v71\n"
      "buffer_store_dwordx4 v[68:71], v59, s[24:27], s78 offen offset:0 sc1\n"
      "v_pk_add_f32 v[132:133], v[132:133], v[148:149]\n"
      "v_pk_add_f32 v[134:135], v[134:135], v[150:151]\n"
      "v_pk_add_f32 v[164:165], v[164:165], v[180:181]\n"
      "v_pk_add_f32 v[166:167], v[166:167], v[182:183]\n"
      "v_pk_add_f32 v[132:133], v[132:133], v[156:157]\n"
      "v_pk_add_f32 v[134:135], v[134:135], v[158:159]\n"
      "v_pk_add_f32 v[164:165], v[164:165], v[188:189]\n"
      "v_pk_add_f32 v[166:167], v[166:167], v[190:191]\n"
      "v_pk_add_f32 v[132:133], v[132:133], v[140:141]\n"
      "v_pk_add_f32 v[134:135], v[134:135], v[142:143]\n"
      "v_pk_add_f32 v[164:165], v[164:165], v[172:173]\n"
      "v_pk_add_f32 v[166:167], v[166:167], v[174:175]\n"
      "v_cvt_pk_bf16_f32 v132, v132, v133\n"
      "v_cvt_pk_bf16_f32 v133, v134, v135\n"
      "v_cvt_pk_bf16_f32 v134, v164, v165\n"
      "v_cvt_pk_bf16_f32 v135, v166, v167\n"
      "s_nop 0\n"
      "v_permlane16_swap_b32 v132, v134\n"
      "v_permlane16_swap_b32 v133, v135\n"
      "buffer_store_dwordx4 v[132:135], v59, s[24:27], s78 offen offset:64 sc1\n"
      "Lk_fin:\n"
      :: "s"(ka), "s"(word), "s"(bid), "v"(tid)
      : "v1","v2","v3","v4","v5","v6","v7","v8","v9","v10","v11","v12","v13","v14","v15","v16","v17","v18","v19","v20","v21","v22","v23","v24","v25","v26","v27","v28","v29","v30","v31","v32","v33","v34","v35","v36","v37","v38","v39","v40","v41","v42","v43","v44","v45","v46","v47","v48","v49","v50","v51","v52","v53","v54","v55","v56","v57","v58","v59","v60","v61","v62","v63","v64","v65","v66","v67","v68","v69","v70","v71","v72","v73","v74","v75","v76","v77","v78","v79","v80","v81","v82","v83","v84","v85","v86","v87","v88","v89","v90","v91","v92","v93","v94","v95","v96","v97","v98","v99","v100","v101","v102","v103","v104","v105","v106","v107","v108","v109","v110","v111","v112","v113","v114","v115","v116","v117","v118","v119","v120","v121","v122","v123","v124","v125","v126","v127","v128","v129","v130","v131","v132","v133","v134","v135","v136","v137","v138","v139","v140","v141","v142","v143","v144","v145","v146","v147","v148","v149","v150","v151","v152","v153","v154","v155","v156","v157","v158","v159","v160","v161","v162","v163","v164","v165","v166","v167","v168","v169","v170","v171","v172","v173","v174","v175","v176","v177","v178","v179","v180","v181","v182","v183","v184","v185","v186","v187","v188","v189","v190","v191","v192","v193","v194","v195","v196","v197","v198","v199","v200","v201","v202","v203","v204","v205","v206","v207","v208","v209","v210","v211","v212","v213","v214","v215","v216","v217","v218","v219","v220","v221","v222","v223","v224","v225","v226","v227","v228","v229","v230","v231","v232","v233","v234","v235","v236","v237","v238","v239","v240","v241","v242","v243","v244","v245","v246","v247","v248","v249","v250","v251","v252","v253","v254","v255","a0","a1","a2","a3","a4","a5","a6","a7","a8","a9","a10","a11","a12","a13","a14","a15","a16","a17","a18","a19","a20","a21","a22","a23","a24","a25","a26","a27","a28","a29","a30","a31","a32","a33","a34","a35","a36","a37","a38","a39","a40","a41","a42","a43","a44","a45","a46","a47","a48","a49","a50","a51","a52","a53","a54","a55","a56","a57","a58","a59","a60","a61","a62","a63","a64","a65","a66","a67","a68","a69","a70","a71","a72","a73","a74","a75","a76","a77","a78","a79","a80","a81","a82","a83","a84","a85","a86","a87","a88","a89","a90","a91","a92","a93","a94","a95","a96","a97","a98","a99","a100","a101","a102","a103","a104","a105","a106","a107","a108","a109","a110","a111","a112","a113","a114","a115","a116","a117","a118","a119","a120","a121","a122","a123","a124","a125","a126","a127","a128","a129","a130","a131","a132","a133","a134","a135","a136","a137","a138","a139","a140","a141","a142","a143","a144","a145","a146","a147","a148","a149","a150","a151","a152","a153","a154","a155","a156","a157","a158","a159","a160","a161","a162","a163","a164","a165","a166","a167","a168","a169","a170","a171","a172","a173","a174","a175","a176","a177","a178","a179","a180","a181","a182","a183","a184","a185","a186","a187","a188","a189","a190","a191","a192","a193","a194","a195","a196","a197","a198","a199","a200","a201","a202","a203","a204","a205","a206","a207","a208","a209","a210","a211","a212","a213","a214","a215","a216","a217","a218","a219","a220","a221","a222","a223","a224","a225","a226","a227","a228","a229","a230","a231","a232","a233","a234","a235","a236","a237","a238","a239","a240","a241","a242","a243","a244","a245","a246","a247","a248","a249","a250","a251","a252","a253","a254","a255","s5","s6","s7","s8","s9","s10","s11","s12","s13","s14","s15","s16","s17","s18","s19","s20","s21","s22","s23","s24","s25","s26","s27","s28","s29","s30","s31","s34","s35","s36","s37","s38","s39","s40","s41","s42","s43","s44","s45","s46","s47","s48","s49","s50","s51","s52","s53","s54","s55","s56","s57","s58","s59","s60","s61","s62","s63","s64","s65","s66","s67","s68","s69","s70","s71","s72","s73","s74","s75","s76","s77","s78","s79","s80","s81","s82","s83","s84","s85","s86","s87","s88","s89","s90","s91","s92","s93","s94","s95","s96","s97","s98","s99","memory","scc","vcc");

}

extern "C" void kernel_launch(void* const* d_in, const int* in_sizes, int n_in, void* d_out, int out_size, void* d_ws, size_t ws_size,
                              hipStream_t stream) {
  (void)in_sizes; (void)n_in; (void)out_size; (void)ws_size;
  (void)hipFuncSetAttribute((const void*)fused_kernel, hipFuncAttributeMaxDynamicSharedMemorySize, 163840);
  const Orders ord = {{0xfedcba9876543120ull, 0xfedcba9876542031ull, 0xfedcba9876532410ull, 0xfedcba9876435201ull, 0xfedcba9875463210ull, 0xfedcba9865743201ull, 0xfedcba9768543210ull, 0xfedcba8796543201ull, 0xfedcb98a76543210ull, 0xfedca9b876543201ull, 0xfedbac9876543210ull, 0xfecbda9876543201ull, 0xfdceba9876543210ull, 0xedfcba9876543201ull, 0xfedcba9876543210ull, 0xfedcba9876543201ull, 0xfedcb98a76543120ull, 0xfedca9b876542031ull, 0xfedbac9876532410ull, 0xfecbda9876435201ull, 0xfdceba9875463210ull, 0xedfcba9865743201ull, 0xfedcba9768543210ull, 0xfedcba8796543201ull, 0xfedba98765c32140ull, 0xfecba98764d03251ull, 0xfdcba9875e410632ull, 0xedcba9865f421703ull, 0xfedcba9765438210ull, 0xfedcba8765409321ull, 0xfedcb987654a1032ull, 0xfedca987654b2103ull, 0x0000000006543120ull, 0x0000000006542031ull, 0x0000000006532410ull, 0x0000000006435201ull, 0x0000000005463210ull, 0x0000000006543201ull, 0x0000000006543210ull, 0x0000000006543201ull, 0x0000000765483210ull, 0x0000000807654321ull, 0x0000000810765432ull, 0x0000000821076543ull, 0x0000000832107654ull, 0x0000000843210765ull, 0x0000000854321076ull, 0x0000000865432107ull, 0x000cb98a76543120ull, 0x000ca9b876542031ull, 0x000bac9876532410ull, 0x000cba9876435201ull, 0x000cba9875463210ull, 0x000cba9865743201ull, 0x000cba9768543210ull, 0x000cba8796543201ull, 0x0edcb98a76543120ull, 0x0edca9b876542031ull, 0x0edbac9876532410ull, 0x0ecbda9876435201ull, 0x0dceba9875463210ull, 0x0edcba9865743201ull, 0x0edcba9768543210ull, 0x0edcba8796543201ull}};
  fused_kernel<<<256, 256, 163840, stream>>>((const u16*)d_in[0], (const u16*)d_in[1], (const u16*)d_in[2], (const u16*)d_in[3],
                                             (const u16*)d_in[4], (const u16*)d_in[6], (const u16*)d_in[7], (u16*)d_out, (char*)d_ws, ord);
}
```
